```python
import torch

def _softmax(x, axis=-1):
    return torch.softmax(x, dim=axis)
def _gelu(x):
    return torch.nn.functional.gelu(x, approximate="tanh")
def _silu(x):
    return torch.nn.functional.silu(x)
def _sigmoid(x):
    return torch.sigmoid(x)
def _layernorm(x, axis=-1, eps=1e-5):
    m = x.mean(dim=axis, keepdim=True)
    v = x.var(dim=axis, keepdim=True, unbiased=False)
    return (x - m) / torch.sqrt(v + eps)
def _scatter_add(tgt, idx, src, axis=0):
    return tgt.clone().index_add_(axis, idx, src)
def _segment_sum(src, seg, n):
    out = torch.zeros((n,) + src.shape[1:], dtype=src.dtype)
    return out.index_add_(0, seg, src)
def _pad(x, pw):
    flat = tuple(v for pair in reversed(pw) for v in pair)
    return torch.nn.functional.pad(x, flat)

kernel_name = 'gated_mix__ssm_conv_s4d__mixer_token_mlp__prompt'

def setup_inputs():

    _w = (lambda a: (lambda t: t.to(torch.bfloat16) if t.is_floating_point() else t)(torch.as_tensor(a)))
    def _A_setup():
        Bsz, L, D, N = 1, 2048, 576, 128
        return {
            "u": _w((lambda *s: torch.randn(*s, dtype=torch.bfloat16) * 0.05)(Bsz, L, D)),
            "log_A_real": _w((lambda *s: torch.randn(*s, dtype=torch.bfloat16) * 3.2)(D, N) * 0.5),
            "A_imag": _w((lambda *s: torch.randn(*s, dtype=torch.bfloat16) * 0.05)(D, N) * 3.14),
            "C_real": _w((lambda *s: torch.randn(*s, dtype=torch.bfloat16) * 0.1)(D, N) / N ** 0.5),
            "C_imag": _w((lambda *s: torch.randn(*s, dtype=torch.bfloat16) * 0.1)(D, N) / N ** 0.5),
            "log_dt": _w((lambda *s: torch.randn(*s, dtype=torch.bfloat16) * 0.05)(D) * 0.5 - 4.0),
            "D_skip": _w((lambda *s: torch.randn(*s, dtype=torch.bfloat16) * 0.05)(D)),
        }
    _A_in = _A_setup()

    def _B_setup():
        B, S, D, F = 1, 2048, 576, 4608
        return {
            "x": _w((lambda *s: torch.randn(*s, dtype=torch.bfloat16) * 0.05)(B, S, D)),
            "w1": _w((lambda *s: torch.randn(*s, dtype=torch.bfloat16) * 0.2)(S, F) * (S ** -0.5)),
            "b1": _w((lambda *s: torch.randn(*s, dtype=torch.bfloat16) * 3.2)(F) * 0.02),
            "w2": _w((lambda *s: torch.randn(*s, dtype=torch.bfloat16) * 0.05)(F, S) * (F ** -0.5)),
            "b2": _w((lambda *s: torch.randn(*s, dtype=torch.bfloat16) * 0.1)(S) * 0.02),
        }
    _B_in = _B_setup()

    return {
        "A_u": _A_in["u"],
        "A_log_A_real": _A_in["log_A_real"],
        "A_A_imag": _A_in["A_imag"],
        "A_C_real": _A_in["C_real"],
        "A_C_imag": _A_in["C_imag"],
        "A_log_dt": _A_in["log_dt"],
        "A_D_skip": _A_in["D_skip"],
        "B_x": _B_in["x"],
        "B_w1": _B_in["w1"],
        "B_b1": _B_in["b1"],
        "B_w2": _B_in["w2"],
        "B_b2": _B_in["b2"],
        "gate_w": _w((lambda *s: torch.randn(*s, dtype=torch.bfloat16) * 0.05)(576) * 16.0),
    }

def reference(A_u, A_log_A_real, A_A_imag, A_C_real, A_C_imag, A_log_dt, A_D_skip, B_x, B_w1, B_b1, B_w2, B_b2, gate_w):
    def _A_ref(u, log_A_real, A_imag, C_real, C_imag, log_dt, D_skip):
        L = u.shape[1]
        dt = torch.exp(log_dt)
        A = -torch.exp(log_A_real) + 1j * A_imag
        C = (C_real + 1j * C_imag) * dt[:, None]
        pos = (lambda x, dt: x.to(dt))(torch.arange(L), log_dt.dtype)
        P = torch.exp((A * dt[:, None])[:, :, None] * pos[None, None, :])
        K = 2.0 * torch.einsum("dn,dnl->dl", C, P).real
        L2 = 2 * L
        u_t = (lambda x, *d: x.permute(*d))(u, 0, 2, 1)
        Kf = (lambda x, *a, **k: torch.fft.rfft(x.float(), *a, **k))(K, L2)
        Uf = (lambda x, *a, **k: torch.fft.rfft(x.float(), *a, **k))(u_t, L2)
        y = (lambda x, *a, **k: torch.fft.irfft(x, *a, **k).to(torch.bfloat16))(Uf * Kf[None, :, :], L2)[:, :, :L]
        y = (lambda x, *d: x.permute(*d))(y, 0, 2, 1)
        return y + u * D_skip[None, None, :]

    def _B_ref(x, w1, b1, w2, b2):
        B, S, D = x.shape
        h = _layernorm(x, axis=-1)
        u = torch.einsum("bsd,sf->bdf", h, w1) + b1
        u = _gelu(u)
        y = torch.einsum("bdf,fs->bds", u, w2) + b2
        return (lambda x, *d: x.permute(*d))(y, 0, 2, 1)

    x = A_u
    g = _sigmoid((x * gate_w).sum(-1, keepdims=True))
    a = _A_ref(x, A_log_A_real, A_A_imag, A_C_real, A_C_imag, A_log_dt, A_D_skip)
    b = _B_ref(x, B_w1, B_b1, B_w2, B_b2)
    return g * a + (1 - g) * b
```

```cpp
#include <hip/hip_runtime.h>
#include <hip/hip_bf16.h>
#include <stdint.h>

typedef unsigned short u16;
__device__ __forceinline__ float bf2f(u16 b){ return __uint_as_float(((unsigned)b)<<16); }
__device__ __forceinline__ u16 f2bf(float f){ unsigned u=__float_as_uint(f); u += 0x7FFFu + ((u>>16)&1u); return (u16)(u>>16); }
__device__ __forceinline__ float rbf(float f){ return bf2f(f2bf(f)); }

constexpr int L=2048, D=576, N=128, F=4608;

__global__ void k_ln(const u16* x, const u16* gw, u16* h, float* g){
  int s=blockIdx.x; int t=threadIdx.x;
  float sum=0, gs=0;
  for(int d=t; d<D; d+=64){ float v=bf2f(x[s*D+d]); sum+=v; gs+=rbf(v*bf2f(gw[d])); }
  for(int o=32;o;o>>=1){ sum+=__shfl_xor(sum,o); gs+=__shfl_xor(gs,o);}
  float mean=sum/D; float vs=0;
  for(int d=t; d<D; d+=64){ float v=bf2f(x[s*D+d])-mean; vs+=v*v; }
  for(int o=32;o;o>>=1){ vs+=__shfl_xor(vs,o);}
  float var=rbf(vs/D); float m=rbf(mean);
  float den=rbf(sqrtf(rbf(var+1e-5f)));
  for(int d=t; d<D; d+=64){ float v=rbf(bf2f(x[s*D+d])-m); h[s*D+d]=f2bf(v/den); }
  if(t==0){ float z=rbf(gs); g[s]=rbf(1.f/(1.f+expf(-z))); }
}
__global__ void k_kgen(const u16* lar,const u16* aim,const u16* cr,const u16* ci,const u16* ldt,float* K){
  int d=blockIdx.y; int l=blockIdx.x*256+threadIdx.x;
  float dt=rbf(expf(bf2f(ldt[d])));
  float pos=rbf((float)l);
  float acc=0;
  for(int n=0;n<N;n++){
    float ar=-rbf(expf(bf2f(lar[d*N+n])))*dt; float ai=bf2f(aim[d*N+n])*dt;
    float Cr=bf2f(cr[d*N+n])*dt, Ci=bf2f(ci[d*N+n])*dt;
    float e=expf(ar*pos); float sn,cs; sincosf(ai*pos,&sn,&cs);
    acc+= Cr*e*cs - Ci*e*sn;
  }
  K[d*L+l]=2.f*acc;
}
__global__ void k_conv(const u16* x,const float* K,const u16* dsk,u16* a){
  int d=blockIdx.y; int l=blockIdx.x*256+threadIdx.x;
  float acc=0;
  for(int m=0;m<=l;m++) acc+=K[d*L+m]*bf2f(x[(l-m)*D+d]);
  float y=rbf(acc); float sk=rbf(bf2f(x[l*D+d])*bf2f(dsk[d]));
  a[l*D+d]=f2bf(y+sk);
}
__global__ void k_g1(const u16* h,const u16* w1,const u16* b1,u16* H){
  int f=blockIdx.x*256+threadIdx.x; int d=blockIdx.y;
  float acc=0;
  for(int s=0;s<L;s++) acc+=bf2f(h[s*D+d])*bf2f(w1[(size_t)s*F+f]);
  float u=rbf(rbf(acc)+bf2f(b1[f]));
  float t=tanhf(0.7978845608028654f*(u+0.044715f*u*u*u));
  H[d*F+f]=f2bf(0.5f*u*(1.f+t));
}
__global__ void k_g2(const u16* H,const u16* w2,const u16* b2,const u16* a,const float* g,u16* out){
  int s=blockIdx.x*256+threadIdx.x; int d=blockIdx.y;
  float acc=0;
  for(int f=0;f<F;f++) acc+=bf2f(H[d*F+f])*bf2f(w2[(size_t)f*L+s]);
  float b=rbf(rbf(acc)+bf2f(b2[s]));
  float gg=g[s];
  float o=rbf(gg*bf2f(a[s*D+d]))+rbf(rbf(1.f-gg)*b);
  out[s*D+d]=f2bf(o);
}

extern "C" void kernel_launch(void* const* d_in, const int* in_sizes, int n_in,
                              void* d_out, int out_size, void* d_ws, size_t ws_size,
                              hipStream_t stream) {
  const u16* x=(const u16*)d_in[0]; const u16* lar=(const u16*)d_in[1]; const u16* aim=(const u16*)d_in[2];
  const u16* cr=(const u16*)d_in[3]; const u16* ci=(const u16*)d_in[4]; const u16* ldt=(const u16*)d_in[5]; const u16* dsk=(const u16*)d_in[6];
  const u16* w1=(const u16*)d_in[8]; const u16* b1=(const u16*)d_in[9]; const u16* w2=(const u16*)d_in[10]; const u16* b2=(const u16*)d_in[11]; const u16* gw=(const u16*)d_in[12];
  char* ws=(char*)d_ws;
  u16* h=(u16*)ws; float* g=(float*)(ws+(4<<20)); float* K=(float*)(ws+(8<<20)); u16* a=(u16*)(ws+(16<<20)); u16* H=(u16*)(ws+(24<<20));
  k_ln<<<L,64,0,stream>>>(x,gw,h,g);
  k_kgen<<<dim3(L/256,D),256,0,stream>>>(lar,aim,cr,ci,ldt,K);
  k_conv<<<dim3(L/256,D),256,0,stream>>>(x,K,dsk,a);
  k_g1<<<dim3(F/256,D),256,0,stream>>>(h,w1,b1,H);
  k_g2<<<dim3(L/256,D),256,0,stream>>>(H,w2,b2,a,g,(u16*)d_out);
}
```

```cpp
#include <hip/hip_runtime.h>
#include <stdint.h>

typedef unsigned short u16;
typedef unsigned int u32;
typedef short bf16x8 __attribute__((ext_vector_type(8)));
typedef short s16x4 __attribute__((ext_vector_type(4)));
typedef float f32x4 __attribute__((ext_vector_type(4)));
typedef unsigned u32x4 __attribute__((ext_vector_type(4)));
typedef unsigned u32x2 __attribute__((ext_vector_type(2)));
typedef __bf16 bf16x2_t __attribute__((ext_vector_type(2)));
#define LAS __attribute__((address_space(3)))
#define DI __device__ __forceinline__

constexpr int L_ = 2048, D_ = 576, N_ = 128, F_ = 4608;
constexpr size_t OFF_HF = 0;
constexpr size_t OFF_UT = OFF_HF + (size_t)36 * 64 * 1024;
constexpr size_t OFF_G = OFF_UT + (size_t)D_ * L_ * 2;
constexpr size_t OFF_KT = OFF_G + 8192;
constexpr size_t OFF_HH = OFF_KT + (size_t)D_ * 8192;
constexpr size_t OFF_AT = OFF_HH + (size_t)36 * 144 * 1024;

DI float bf2f(u16 b) { return __uint_as_float(((u32)b) << 16); }
DI float bflo(u32 w) { return __uint_as_float(w << 16); }
DI float bfhi(u32 w) { return __uint_as_float(w & 0xffff0000u); }
DI u32 pk2(float a, float b) { bf16x2_t v = {(__bf16)a, (__bf16)b}; return __builtin_bit_cast(u32, v); }
DI float rbf(float x) { return (float)(__bf16)x; }
DI f32x4 mfma_bf(bf16x8 a, bf16x8 b, f32x4 c) { return __builtin_amdgcn_mfma_f32_16x16x32_bf16(a, b, c, 0, 0, 0); }

constexpr int LN_PITCH = 1168;
DI void ln_block(int b, const u16* __restrict__ x, const u16* __restrict__ gw, char* __restrict__ ws, char* lds) {
  const int tid = threadIdx.x, row = tid >> 3, part = tid & 7;
  const int s0 = b * 32;
  u32x4 v[9], w[9];
  const u32x4* xr = (const u32x4*)(x + (size_t)(s0 + row) * D_);
  const u32x4* gr = (const u32x4*)gw;
#pragma unroll
  for (int i = 0; i < 9; ++i) { v[i] = xr[part + 8 * i]; w[i] = gr[part + 8 * i]; }
  float sum = 0.f, gs = 0.f;
#pragma unroll
  for (int i = 0; i < 9; ++i)
#pragma unroll
    for (int j = 0; j < 4; ++j) {
      float a = bflo(v[i][j]), c = bfhi(v[i][j]);
      sum += a + c;
      gs += rbf(a * bflo(w[i][j])) + rbf(c * bfhi(w[i][j]));
    }
#pragma unroll
  for (int o = 1; o < 8; o <<= 1) { sum += __shfl_xor(sum, o); gs += __shfl_xor(gs, o); }
  const float mean = sum * (1.f / D_);
  float vs = 0.f;
#pragma unroll
  for (int i = 0; i < 9; ++i)
#pragma unroll
    for (int j = 0; j < 4; ++j) {
      float a = bflo(v[i][j]) - mean, c = bfhi(v[i][j]) - mean;
      vs += a * a + c * c;
    }
#pragma unroll
  for (int o = 1; o < 8; o <<= 1) vs += __shfl_xor(vs, o);
  const float var = rbf(vs * (1.f / D_)), m = rbf(mean);
  const float den = rbf(sqrtf(rbf(var + 1e-5f)));
  if (part == 0) {
    float z = rbf(gs);
    ((float*)(ws + OFF_G))[s0 + row] = rbf(1.f / (1.f + __expf(-z)));
  }
  const int lane = tid & 63, wv = tid >> 6, gq = lane >> 4, c = lane & 15;
#pragma unroll
  for (int pass = 0; pass < 2; ++pass) {
    if (pass) __syncthreads();
#pragma unroll
    for (int i = 0; i < 9; ++i) {
      u32x4 o = v[i];
      if (pass) {
#pragma unroll
        for (int j = 0; j < 4; ++j) o[j] = pk2(rbf(bflo(v[i][j]) - m) / den, rbf(bfhi(v[i][j]) - m) / den);
      }
      *(u32x4*)(lds + row * LN_PITCH + (part + 8 * i) * 16) = o;
    }
    __syncthreads();
    for (int dt = wv; dt < 36; dt += 4) {
      u16 e[8];
      if (pass == 0) {
#pragma unroll
        for (int jj = 0; jj < 8; ++jj) e[jj] = *(const u16*)(lds + (8 * gq + jj) * LN_PITCH + (16 * dt + c) * 2);
      } else {
#pragma unroll
        for (int jj = 0; jj < 8; ++jj) e[jj] = *(const u16*)(lds + (16 * (jj >> 2) + 4 * gq + (jj & 3)) * LN_PITCH + (16 * dt + c) * 2);
      }
      u32x4 o;
#pragma unroll
      for (int j = 0; j < 4; ++j) o[j] = (u32)e[2 * j] | ((u32)e[2 * j + 1] << 16);
      if (pass == 0) *(u32x4*)(ws + OFF_UT + (size_t)(16 * dt + c) * 4096 + s0 * 2 + gq * 16) = o;
      else *(u32x4*)(ws + OFF_HF + ((size_t)(dt * 64 + b) * 64 + lane) * 16) = o;
    }
  }
}

DI void kgen_block(int d, const u16* __restrict__ lar, const u16* __restrict__ aim, const u16* __restrict__ cr,
                   const u16* __restrict__ ci, const u16* __restrict__ ldt, char* __restrict__ ws, char* lds) {
  const int tid = threadIdx.x, lane = tid & 63, wv = tid >> 6, gq = lane >> 4, c = lane & 15;
  f32x4* par = (f32x4*)lds;
  f32x4* red = (f32x4*)(lds + 2048);
  float* T = (float*)(lds + 2048 + 12288);
  if (tid < N_) {
    const float dt = rbf(__expf(bf2f(ldt[d])));
    const float ar = -rbf(__expf(bf2f(lar[d * N_ + tid]))) * dt;
    const float ai = bf2f(aim[d * N_ + tid]) * dt;
    const float Cr = bf2f(cr[d * N_ + tid]) * dt, Ci = bf2f(ci[d * N_ + tid]) * dt;
    f32x4 p;
    p[0] = ar * 1.4426950408889634f;
    p[1] = ai * 0.15915494309189535f;
    p[2] = 0.5f * __log2f(Cr * Cr + Ci * Ci) + 1.0f;
    p[3] = atan2f(Ci, Cr) * 0.15915494309189535f;
    par[tid] = p;
  }
  __syncthreads();
  float h0 = (float)(1 + (c & 7) + 128 * (c >> 3));
  float h1 = (c < 8) ? (float)(258 + 2 * (c & 3) + 128 * ((c >> 2) & 1)) : (float)(516 + 4 * (c & 1) + 128 * ((c - 8) >> 1));
  float h2 = (c < 8) ? (float)(1032 + 128 * c) : 0.f;
  float lc = (float)(8 * c);
  f32x4 acc0 = {0, 0, 0, 0}, acc1 = acc0, acc2 = acc0;
#pragma unroll 2
  for (int t = 0; t < 8; ++t) {
    const f32x4 p = par[32 * wv + 4 * t + gq];
    float bm = __builtin_amdgcn_exp2f(p[0] * lc), bp = __builtin_amdgcn_fractf(p[1] * lc);
    float br = bm * __builtin_amdgcn_cosf(bp), bi = bm * __builtin_amdgcn_sinf(bp);
    {
      float m = __builtin_amdgcn_exp2f(fmaf(p[0], h0, p[2])), ph = __builtin_amdgcn_fractf(fmaf(p[1], h0, p[3]));
      acc0 = __builtin_amdgcn_mfma_f32_16x16x4f32(m * __builtin_amdgcn_cosf(ph), br, acc0, 0, 0, 0);
      acc0 = __builtin_amdgcn_mfma_f32_16x16x4f32(-m * __builtin_amdgcn_sinf(ph), bi, acc0, 0, 0, 0);
    }
    {
      float m = __builtin_amdgcn_exp2f(fmaf(p[0], h1, p[2])), ph = __builtin_amdgcn_fractf(fmaf(p[1], h1, p[3]));
      acc1 = __builtin_amdgcn_mfma_f32_16x16x4f32(m * __builtin_amdgcn_cosf(ph), br, acc1, 0, 0, 0);
      acc1 = __builtin_amdgcn_mfma_f32_16x16x4f32(-m * __builtin_amdgcn_sinf(ph), bi, acc1, 0, 0, 0);
    }
    {
      float m = __builtin_amdgcn_exp2f(fmaf(p[0], h2, p[2])), ph = __builtin_amdgcn_fractf(fmaf(p[1], h2, p[3]));
      acc2 = __builtin_amdgcn_mfma_f32_16x16x4f32(m * __builtin_amdgcn_cosf(ph), br, acc2, 0, 0, 0);
      acc2 = __builtin_amdgcn_mfma_f32_16x16x4f32(-m * __builtin_amdgcn_sinf(ph), bi, acc2, 0, 0, 0);
    }
  }
  red[(wv * 3 + 0) * 64 + lane] = acc0;
  red[(wv * 3 + 1) * 64 + lane] = acc1;
  red[(wv * 3 + 2) * 64 + lane] = acc2;
  __syncthreads();
  if (tid < 192) {
    const int k = wv;
    f32x4 s = red[(0 * 3 + k) * 64 + lane] + red[(1 * 3 + k) * 64 + lane] + red[(2 * 3 + k) * 64 + lane] + red[(3 * 3 + k) * 64 + lane];
#pragma unroll
    for (int i = 0; i < 4; ++i) {
      const int r = 4 * gq + i;
      int hp;
      if (k == 0) hp = 1 + (r & 7) + 128 * (r >> 3);
      else if (k == 1) hp = (r < 8) ? 258 + 2 * (r & 3) + 128 * ((r >> 2) & 1) : 516 + 4 * (r & 1) + 128 * ((r - 8) >> 1);
      else hp = (r < 8) ? 1032 + 128 * r : 0;
      const int p = hp + 8 * c;
      if (k < 2 || r < 8 || (r == 8 && c == 0)) T[p] = s[i];
    }
  }
  __syncthreads();
  {
    const int j = tid >> 2, q = tid & 3;
    u32x4 hi, lo;
#pragma unroll
    for (int e = 0; e < 4; ++e) {
      const int m0 = 32 * j + 31 - (8 * q + 2 * e), m1 = m0 - 1;
      const float k0 = T[(int)rbf((float)m0)], k1 = T[(int)rbf((float)m1)];
      const float a0 = rbf(k0), a1 = rbf(k1);
      hi[e] = pk2(a0, a1);
      lo[e] = pk2(k0 - a0, k1 - a1);
    }
    char* kt = ws + OFF_KT + (size_t)d * 8192 + j * 128 + q * 16;
    *(u32x4*)kt = hi;
    *(u32x4*)(kt + 64) = lo;
  }
}

__global__ __launch_bounds__(256) void k1_pre(const u16* __restrict__ x, const u16* __restrict__ gw, const u16* __restrict__ lar,
                                              const u16* __restrict__ aim, const u16* __restrict__ cr, const u16* __restrict__ ci,
                                              const u16* __restrict__ ldt, char* __restrict__ ws) {
  __shared__ __attribute__((aligned(16))) char lds[32 * LN_PITCH];
  if (blockIdx.x < 64) ln_block(blockIdx.x, x, gw, ws, lds);
  else kgen_block(blockIdx.x - 64, lar, aim, cr, ci, ldt, ws, lds);
}

constexpr int UC_STRIDE = 4160, KR_OFF = 8 * UC_STRIDE, KR_STRIDE = 160, CONV_LDS = KR_OFF + 80 * KR_STRIDE;
DI void conv_block(int d, const u16* __restrict__ dsk, char* __restrict__ ws, char* lds) {
  const int tid = threadIdx.x, lane = tid & 63, wv = tid >> 6, gq = lane >> 4, c = lane & 15;
  const u32x4* ut = (const u32x4*)(ws + OFF_UT + (size_t)d * 4096);
  const u32x4* kt = (const u32x4*)(ws + OFF_KT + (size_t)d * 8192);
  const u32x4 z4 = {0, 0, 0, 0};
  u32x4 c0 = ut[tid], c1 = (tid < 255) ? ut[tid + 1] : z4;
  u32x4 k0 = kt[tid], k1 = kt[tid + 256];
  {
    *(u32x4*)(lds + KR_OFF + (16 + (tid >> 3)) * KR_STRIDE + (tid & 7) * 16) = k0;
    *(u32x4*)(lds + KR_OFF + (16 + 32 + (tid >> 3)) * KR_STRIDE + (tid & 7) * 16) = k1;
    if (tid < 128) *(u32x4*)(lds + KR_OFF + (tid >> 3) * KR_STRIDE + (tid & 7) * 16) = z4;
  }
  auto put = [&](u32x4 a, u32x4 b, int X) {
    u32 e[8] = {a[0], a[1], a[2], a[3], b[0], b[1], b[2], b[3]};
#pragma unroll
    for (int cp = 0; cp < 8; ++cp) {
      const int sh = cp + 1;
      u32x4 o;
#pragma unroll
      for (int j = 0; j < 4; ++j) {
        if (sh & 1) o[j] = __builtin_amdgcn_alignbyte(e[(sh >> 1) + j + 1], e[(sh >> 1) + j], 2);
        else o[j] = (sh == 8) ? e[4 + j] : e[(sh >> 1) + j];
      }
      *(u32x4*)(lds + cp * UC_STRIDE + X * 16) = o;
    }
  };
  put(c0, c1, tid + 4);
  if (tid < 4) put(z4, (tid == 3) ? ut[0] : z4, tid);
  __syncthreads();
  f32x4 acc[4][2];
#pragma unroll
  for (int a = 0; a < 4; ++a) { acc[a][0] = f32x4{0, 0, 0, 0}; acc[a][1] = f32x4{0, 0, 0, 0}; }
  const char* abase = lds + (c & 7) * UC_STRIDE + (8 * gq + (c & 8)) * 2;
  const char* bbase = lds + KR_OFF + (16 + c) * KR_STRIDE + gq * 16;
#pragma unroll 1
  for (int it = 0; it < 4; ++it) {
    const int d0 = wv * 4 + it;
    bf16x8 A[4][2], Bh[4], Bl[4];
#pragma unroll
    for (int k = 0; k < 4; ++k)
#pragma unroll
      for (int rt = 0; rt < 2; ++rt) A[k][rt] = *(const bf16x8*)(abase + (32 * (d0 + 16 * k) + 16 * rt) * 2);
#pragma unroll
    for (int m = 0; m < 4; ++m) {
      Bh[m] = *(const bf16x8*)(bbase + (16 * m - d0) * KR_STRIDE);
      Bl[m] = *(const bf16x8*)(bbase + (16 * m - d0) * KR_STRIDE + 64);
    }
#pragma unroll
    for (int k = 0; k < 4; ++k)
#pragma unroll
      for (int ct = k; ct < 4; ++ct)
#pragma unroll
        for (int rt = 0; rt < 2; ++rt) {
          acc[ct][rt] = mfma_bf(A[k][rt], Bh[ct - k], acc[ct][rt]);
          acc[ct][rt] = mfma_bf(A[k][rt], Bl[ct - k], acc[ct][rt]);
        }
  }
  __syncthreads();
  f32x4* red = (f32x4*)lds;
#pragma unroll
  for (int ct = 0; ct < 4; ++ct)
#pragma unroll
    for (int rt = 0; rt < 2; ++rt) red[(wv * 8 + ct * 2 + rt) * 64 + lane] = acc[ct][rt];
  __syncthreads();
  const float dskv = bf2f(dsk[d]);
#pragma unroll
  for (int h = 0; h < 2; ++h) {
    const int fr = wv * 2 + h;
    const int ct = fr >> 1, rt = fr & 1;
    f32x4 s = red[(0 * 8 + fr) * 64 + lane] + red[(1 * 8 + fr) * 64 + lane] + red[(2 * 8 + fr) * 64 + lane] + red[(3 * 8 + fr) * 64 + lane];
    const int l = 32 * (16 * ct + c) + 16 * rt + 4 * gq;
    const u32x2 uu = *(const u32x2*)(ws + OFF_UT + (size_t)d * 4096 + l * 2);
    float o0 = rbf(s[0]) + rbf(bflo(uu[0]) * dskv), o1 = rbf(s[1]) + rbf(bfhi(uu[0]) * dskv);
    float o2 = rbf(s[2]) + rbf(bflo(uu[1]) * dskv), o3 = rbf(s[3]) + rbf(bfhi(uu[1]) * dskv);
    u32x2 o = {pk2(o0, o1), pk2(o2, o3)};
    *(u32x2*)(ws + OFF_AT + (size_t)d * 4096 + l * 2) = o;
  }
}

__global__ __launch_bounds__(256) void k_conv_(const u16* __restrict__ dsk, char* __restrict__ ws) {
  __shared__ __attribute__((aligned(16))) char lds[CONV_LDS];
  conv_block(blockIdx.x, dsk, ws, lds);
}

template <int NT, int KT, int PD>
struct GemmCore {
};

#define G1_PD 4
#define G2_PD 8

template <int NT, int KT, int PD, bool W_IS_A>
DI void gemm_core(const u16* __restrict__ W, int ldw, const char* __restrict__ fr, size_t fr_tstride, int t0, bool stager,
                  char* lds, f32x4 (&acc)[4][NT]) {
  const int tid = threadIdx.x, lane = tid & 63, gq = lane >> 4, q = (lane & 15) >> 2, p = lane & 3;
  const int srow = (tid >> 3) & 31, sch = tid & 7;
  const int sw0 = (srow >> 1) & 3;
  const int st_off = srow * 128 + ((sch ^ (sw0 << 1)) * 16);
  const u16* wsrc = W + (size_t)srow * ldw + sch * 8;
  const int fsw = (q >> 1) | ((gq & 1) << 1);
  const int rd_base = (4 * gq + q) * 128 + 8 * p;
  const char* frl = fr + lane * 16;
  u32x4 wr[PD][2];
  bf16x8 hb[PD][NT][2];
  auto ldw_tile = [&](int t, u32x4 (&r)[2]) {
    const int tt = (t + t0) % KT;
    if (stager) {
      r[0] = *(const u32x4*)(wsrc + (size_t)(tt * 64) * ldw);
      r[1] = *(const u32x4*)(wsrc + (size_t)(tt * 64 + 32) * ldw);
    }
  };
  auto ldh_tile = [&](int t, bf16x8 (&r)[NT][2]) {
    const int tt = (t + t0) % KT;
#pragma unroll
    for (int b = 0; b < NT; ++b) {
      r[b][0] = *(const bf16x8*)(frl + b * fr_tstride + (size_t)(tt * 2) * 1024);
      r[b][1] = *(const bf16x8*)(frl + b * fr_tstride + (size_t)(tt * 2 + 1) * 1024);
    }
  };
  auto stw_tile = [&](int buf, u32x4 (&r)[2]) {
    if (stager) {
      *(u32x4*)(lds + buf * 8192 + st_off) = r[0];
      *(u32x4*)(lds + buf * 8192 + 4096 + st_off) = r[1];
    }
  };
#pragma unroll
  for (int i = 0; i < PD; ++i) { ldw_tile(i, wr[i]); ldh_tile(i, hb[i]); }
  stw_tile(0, wr[0]);
  ldw_tile(PD, wr[0]);
  __syncthreads();
  static_assert(KT % PD == 0, "KT % PD");
  static_assert(PD % 2 == 0, "PD even");
  for (int tb = 0; tb < KT; tb += PD) {
#pragma unroll
    for (int i = 0; i < PD; ++i) {
      const int t = tb + i;
      const int cur = i & 1;
      if (t + 1 < KT) {
        stw_tile(cur ^ 1, wr[(i + 1) % PD]);
        if (t + 1 + PD < KT) ldw_tile(t + 1 + PD, wr[(i + 1) % PD]);
      }
#pragma unroll
      for (int ks = 0; ks < 2; ++ks) {
        bf16x8 wf[4];
#pragma unroll
        for (int a = 0; a < 4; ++a) {
          const char* ad = lds + cur * 8192 + ks * 4096 + rd_base + ((a ^ fsw) * 32);
          s16x4 lo = __builtin_amdgcn_ds_read_tr16_b64_v4i16((LAS s16x4*)(ad));
          s16x4 hi = __builtin_amdgcn_ds_read_tr16_b64_v4i16((LAS s16x4*)(ad + 2048));
          wf[a] = __builtin_shufflevector(lo, hi, 0, 1, 2, 3, 4, 5, 6, 7);
        }
#pragma unroll
        for (int a = 0; a < 4; ++a)
#pragma unroll
          for (int b = 0; b < NT; ++b)
            acc[a][b] = W_IS_A ? mfma_bf(wf[a], hb[i][b][ks], acc[a][b]) : mfma_bf(hb[i][b][ks], wf[a], acc[a][b]);
      }
      if (t + PD < KT) ldh_tile(t + PD, hb[i]);
      __syncthreads();
    }
  }
}

DI float gelu_t(float u) {
  const float z = 0.7978845608028654f * (u + 0.044715f * u * u * u);
  const float e = __expf(2.f * z);
  const float t = 1.f - 2.f / (e + 1.f);
  return 0.5f * u * (1.f + t);
}

DI void gemm1_block(int bid, const u16* __restrict__ w1, const u16* __restrict__ b1, char* __restrict__ ws, char* lds) {
  const int xcd = bid & 7, loc = bid >> 3;
  const int ft = xcd * 9 + loc / 3, dg = loc % 3;
  const int tid = threadIdx.x, lane = tid & 63, wv = tid >> 6, gq = lane >> 4;
  const int dt0 = dg * 12 + wv * 3;
  f32x4 acc[4][3];
#pragma unroll
  for (int a = 0; a < 4; ++a)
#pragma unroll
    for (int b = 0; b < 3; ++b) acc[a][b] = f32x4{0, 0, 0, 0};
#define G1_STAG 0
  const int t0 = G1_STAG ? (loc * G1_STAG) % 32 : 0;
  gemm_core<3, 32, G1_PD, true>(w1 + ft * 64, F_, ws + OFF_HF + (size_t)dt0 * 64 * 1024, (size_t)64 * 1024, t0, true, lds, acc);
#pragma unroll
  for (int pp = 0; pp < 2; ++pp) {
    const u32x2 ba = *(const u32x2*)(b1 + ft * 64 + 32 * pp + 4 * gq);
    const u32x2 bb = *(const u32x2*)(b1 + ft * 64 + 32 * pp + 16 + 4 * gq);
    const float bia[4] = {bflo(ba[0]), bfhi(ba[0]), bflo(ba[1]), bfhi(ba[1])};
    const float bib[4] = {bflo(bb[0]), bfhi(bb[0]), bflo(bb[1]), bfhi(bb[1])};
#pragma unroll
    for (int b = 0; b < 3; ++b) {
      float ra[4], rb[4];
#pragma unroll
      for (int i = 0; i < 4; ++i) {
        ra[i] = gelu_t(rbf(rbf(acc[2 * pp][b][i]) + bia[i]));
        rb[i] = gelu_t(rbf(rbf(acc[2 * pp + 1][b][i]) + bib[i]));
      }
      u32x4 o = {pk2(ra[0], ra[1]), pk2(ra[2], ra[3]), pk2(rb[0], rb[1]), pk2(rb[2], rb[3])};
      *(u32x4*)(ws + OFF_HH + ((size_t)((dt0 + b) * 144 + ft * 2 + pp) * 64 + lane) * 16) = o;
    }
  }
}

__global__ __launch_bounds__(256) void k2_gemm1(const u16* __restrict__ w1, const u16* __restrict__ b1, char* __restrict__ ws) {
  __shared__ __attribute__((aligned(16))) char lds[16384];
  gemm1_block(blockIdx.x, w1, b1, ws, lds);
}

__global__ __launch_bounds__(320) void k3_gemm2(const u16* __restrict__ w2, const u16* __restrict__ b2, char* __restrict__ ws,
                                                u16* __restrict__ out) {
  __shared__ __attribute__((aligned(16))) char lds[16384];
  const int bid = blockIdx.x;
  const int xcd = bid & 7, loc = bid >> 3;
  const int st = xcd * 4 + (loc >> 3), dgi = loc & 7;
  const int dtb = (dgi < 4) ? dgi * 5 : 20 + (dgi - 4) * 4;
  const int ndt = (dgi < 4) ? 5 : 4;
  const int tid = threadIdx.x, lane = tid & 63, wv = tid >> 6, gq = lane >> 4, c = lane & 15;
  const bool active = wv < ndt;
  const int dt = active ? dtb + wv : dtb;
  f32x4 acc[4][1];
#pragma unroll
  for (int a = 0; a < 4; ++a) acc[a][0] = f32x4{0, 0, 0, 0};
#define G2_STAG 0
  const int t0 = G2_STAG ? (loc * G2_STAG) % 72 : 0;
  gemm_core<1, 72, G2_PD, false>(w2 + st * 64, L_, ws + OFF_HH + (size_t)dt * 144 * 1024, 0, t0, wv < 4, lds, acc);
  if (!active) return;
  const float* g = (const float*)(ws + OFF_G);
  const u16* aT = (const u16*)(ws + OFF_AT);
#pragma unroll
  for (int a = 0; a < 4; ++a) {
    const int s = st * 64 + 16 * a + c;
    const float gg = g[s], og = rbf(1.f - gg), bb = bf2f(b2[s]);
    float o[4];
#pragma unroll
    for (int i = 0; i < 4; ++i) {
      const int d = 16 * dt + 4 * gq + i;
      const float av = bf2f(aT[(size_t)d * L_ + s]);
      const float bv = rbf(rbf(acc[a][0][i]) + bb);
      o[i] = rbf(gg * av) + rbf(og * bv);
    }
    u32x2 ov = {pk2(o[0], o[1]), pk2(o[2], o[3])};
    *(u32x2*)(out + (size_t)s * D_ + 16 * dt + 4 * gq) = ov;
  }
}

extern "C" void kernel_launch(void* const* d_in, const int* in_sizes, int n_in, void* d_out, int out_size, void* d_ws,
                              size_t ws_size, hipStream_t stream) {
  const u16* x = (const u16*)d_in[0];
  const u16* lar = (const u16*)d_in[1];
  const u16* aim = (const u16*)d_in[2];
  const u16* cr = (const u16*)d_in[3];
  const u16* ci = (const u16*)d_in[4];
  const u16* ldt = (const u16*)d_in[5];
  const u16* dsk = (const u16*)d_in[6];
  const u16* w1 = (const u16*)d_in[8];
  const u16* b1 = (const u16*)d_in[9];
  const u16* w2 = (const u16*)d_in[10];
  const u16* b2 = (const u16*)d_in[11];
  const u16* gw = (const u16*)d_in[12];
  char* ws = (char*)d_ws;
  k1_pre<<<64 + 576, 256, 0, stream>>>(x, gw, lar, aim, cr, ci, ldt, ws);
  k_conv_<<<576, 256, 0, stream>>>(dsk, ws);
  k2_gemm1<<<216, 256, 0, stream>>>(w1, b1, ws);
  k3_gemm2<<<256, 320, 0, stream>>>(w2, b2, ws, (u16*)d_out);
}
```

```cpp
#include <hip/hip_runtime.h>
#include <stdint.h>

typedef unsigned short u16;
typedef unsigned int u32;
typedef short bf16x8 __attribute__((ext_vector_type(8)));
typedef short s16x4 __attribute__((ext_vector_type(4)));
typedef float f32x4 __attribute__((ext_vector_type(4)));
typedef unsigned u32x4 __attribute__((ext_vector_type(4)));
typedef unsigned u32x2 __attribute__((ext_vector_type(2)));
typedef __bf16 bf16x2_t __attribute__((ext_vector_type(2)));
#define LAS __attribute__((address_space(3)))
#define GAS __attribute__((address_space(1)))
#define DI __device__ __forceinline__
typedef GAS unsigned gu32;
#define RLX_AGENT __ATOMIC_RELAXED, __HIP_MEMORY_SCOPE_AGENT

constexpr int L_ = 2048, D_ = 576, N_ = 128, F_ = 4608;
constexpr size_t OFF_CTL = 0;
constexpr size_t OFF_HF = 65536;
constexpr size_t OFF_XF = OFF_HF + (size_t)36 * 64 * 1024;
constexpr size_t OFF_G = OFF_XF + (size_t)36 * 64 * 1024;
constexpr size_t OFF_KT = OFF_G + 8192;
constexpr size_t OFF_HH = OFF_KT + (size_t)D_ * 8192;
constexpr size_t OFF_AT = OFF_HH + (size_t)36 * 144 * 1024;
constexpr size_t WS_USED = OFF_AT + (size_t)D_ * L_ * 2;
constexpr int NREP = 16;
constexpr int W_HFC = 0;
constexpr int W_AC = 64 * NREP;
constexpr int W_TMO = 64 * 2 * NREP;
constexpr int CTL_WORDS = 64 * 2 * NREP + 64;

#define G1_R 7
#define G2_RW 2
#define G2_RH 4
#define W1_AUX 0
#define W2_AUX 0
#define POLL_SLEEP 4
#define PF2_ROWS 1024
#define PF_AUX 0
#define PF2_START 8
#define W_AUX 2
#define SB() __builtin_amdgcn_sched_barrier(0)
#define TC(ws, k)
#define TS(ws, k)
#define TS1(ws, k)

DI float bf2f(u16 b) { return __uint_as_float(((u32)b) << 16); }
DI float bflo(u32 w) { return __uint_as_float(w << 16); }
DI float bfhi(u32 w) { return __uint_as_float(w & 0xffff0000u); }
DI u32 pk2(float a, float b) { bf16x2_t v = {(__bf16)a, (__bf16)b}; return __builtin_bit_cast(u32, v); }
DI float rbf(float x) { return (float)(__bf16)x; }
DI f32x4 mfma_bf(bf16x8 a, bf16x8 b, f32x4 c) { return __builtin_amdgcn_mfma_f32_16x16x32_bf16(a, b, c, 0, 0, 0); }
DI __amdgpu_buffer_rsrc_t mk_rsrc(const void* p, unsigned bytes) { return __builtin_amdgcn_make_buffer_rsrc((void*)p, 0, bytes, 0x00020000); }
template <int CTRL, int RM>
DI float dpp_add(float v) {
  return v + __builtin_bit_cast(float, __builtin_amdgcn_update_dpp(0, __builtin_bit_cast(int, v), CTRL, RM, 0xf, false));
}
DI float wave_sum(float v) {
  v = dpp_add<0x111, 0xf>(v); v = dpp_add<0x112, 0xf>(v); v = dpp_add<0x114, 0xf>(v); v = dpp_add<0x118, 0xf>(v);
  v = dpp_add<0x142, 0xa>(v); v = dpp_add<0x143, 0xc>(v);
  return __builtin_bit_cast(float, __builtin_amdgcn_readlane(__builtin_bit_cast(int, v), 63));
}

constexpr int LN_PITCH = 1168;
DI void ln_block(int b, const u16* __restrict__ x, const u16* __restrict__ gw, char* __restrict__ ws, char* lds) {
  const int tid = threadIdx.x, lane = tid & 63, wv = tid >> 6;
  const u32x4 z4 = {0, 0, 0, 0};
  const u32x4* gr = (const u32x4*)gw;
  u32x4 v[2][2], w[2];
#pragma unroll
  for (int r = 0; r < 2; ++r) {
    const u32x4* xr = (const u32x4*)(x + (size_t)(8 * b + 2 * wv + r) * D_);
    v[r][0] = xr[lane];
    v[r][1] = (lane < 8) ? xr[64 + lane] : z4;
  }
  w[0] = gr[lane]; w[1] = (lane < 8) ? gr[64 + lane] : z4;
#pragma unroll
  for (int r = 0; r < 2; ++r) {
    const int row = 8 * b + 2 * wv + r;
    float sum = 0.f, gs = 0.f;
#pragma unroll
    for (int i = 0; i < 2; ++i)
#pragma unroll
      for (int j = 0; j < 4; ++j) {
        float a = bflo(v[r][i][j]), c = bfhi(v[r][i][j]);
        sum += a + c;
        gs += rbf(a * bflo(w[i][j])) + rbf(c * bfhi(w[i][j]));
      }
    sum = wave_sum(sum); gs = wave_sum(gs);
    const float mean = sum * (1.f / D_);
    float vs = 0.f;
#pragma unroll
    for (int j = 0; j < 4; ++j) {
      float a = bflo(v[r][0][j]) - mean, c = bfhi(v[r][0][j]) - mean;
      vs += a * a + c * c;
    }
    if (lane < 8) {
#pragma unroll
      for (int j = 0; j < 4; ++j) {
        float a = bflo(v[r][1][j]) - mean, c = bfhi(v[r][1][j]) - mean;
        vs += a * a + c * c;
      }
    }
    vs = wave_sum(vs);
    const float var = rbf(vs * (1.f / D_)), m = rbf(mean);
    const float den = rbf(sqrtf(rbf(var + 1e-5f)));
    const float rden = 1.f / den;
    if (lane == 0) {
      float z = rbf(gs);
      ((float*)(ws + OFF_G))[row] = rbf(1.f / (1.f + __expf(-z)));
    }
#pragma unroll
    for (int i = 0; i < 2; ++i) {
      if (i == 0 || lane < 8) {
        u32x4 o;
#pragma unroll
        for (int j = 0; j < 4; ++j) o[j] = pk2(rbf(bflo(v[r][i][j]) - m) * rden, rbf(bfhi(v[r][i][j]) - m) * rden);
        *(u32x4*)(lds + (2 * wv + r) * LN_PITCH + (64 * i + lane) * 16) = o;
        *(u32x4*)(lds + (8 + 2 * wv + r) * LN_PITCH + (64 * i + lane) * 16) = v[r][i];
      }
    }
  }
  __syncthreads();
  for (int pc = tid; pc < 2 * D_; pc += 256) {
    const int which = pc >= D_, d = which ? pc - D_ : pc;
    const char* src = lds + which * 8 * LN_PITCH + d * 2;
    u16 e[8];
#pragma unroll
    for (int r = 0; r < 8; ++r) e[r] = *(const u16*)(src + r * LN_PITCH);
    u32x4 o;
#pragma unroll
    for (int j = 0; j < 4; ++j) o[j] = (u32)e[2 * j] | ((u32)e[2 * j + 1] << 16);
    *(u32x4*)(ws + (which ? OFF_XF : OFF_HF) + (size_t)(d >> 4) * 65536 + b * 256 + (d & 15) * 16) = o;
  }
}

constexpr int KG_LDS = 2048 + 12288 + 8208;
DI void kgen_block(int d, const u16* __restrict__ lar, const u16* __restrict__ aim, const u16* __restrict__ cr,
                   const u16* __restrict__ ci, const u16* __restrict__ ldt, char* __restrict__ ws, char* lds) {
  const int tid = threadIdx.x, lane = tid & 63, wv = tid >> 6, gq = lane >> 4, c = lane & 15;
  f32x4* par = (f32x4*)lds;
  f32x4* red = (f32x4*)(lds + 2048);
  float* T = (float*)(lds + 2048 + 12288);
  if (lane < 32) {
    const int n = d * N_ + 32 * wv + lane;
    const u16 vlar = lar[n], vaim = aim[n], vcr = cr[n], vci = ci[n], vldt = ldt[d];
    const float dt = rbf(__expf(bf2f(vldt)));
    f32x4 p;
    p[0] = -rbf(__expf(bf2f(vlar))) * dt * 1.4426950408889634f;
    p[1] = bf2f(vaim) * dt * 0.15915494309189535f;
    p[2] = 2.f * (bf2f(vcr) * dt);
    p[3] = 2.f * (bf2f(vci) * dt);
    par[32 * wv + lane] = p;
  }
  __builtin_amdgcn_fence(__ATOMIC_RELEASE, "wavefront");
  __builtin_amdgcn_wave_barrier();
  {
    float hh[3];
    hh[0] = (float)(1 + (c & 7) + 128 * (c >> 3));
    hh[1] = (c < 8) ? (float)(258 + 2 * (c & 3) + 128 * ((c >> 2) & 1)) : (float)(516 + 4 * (c & 1) + 128 * ((c - 8) >> 1));
    hh[2] = (c < 8) ? (float)(1032 + 128 * c) : 0.f;
    const float lc = (float)(8 * c);
    f32x4 acc[3] = {{0, 0, 0, 0}, {0, 0, 0, 0}, {0, 0, 0, 0}};
#pragma unroll 2
    for (int t = 0; t < 8; ++t) {
      const f32x4 p = par[32 * wv + 4 * t + gq];
      const float bm = __builtin_amdgcn_exp2f(p[0] * lc), bp = __builtin_amdgcn_fractf(p[1] * lc);
      const float er = bm * __builtin_amdgcn_cosf(bp), ei = bm * __builtin_amdgcn_sinf(bp);
      const float br = fmaf(p[2], er, -p[3] * ei), bi = fmaf(p[2], ei, p[3] * er);
      float ac[3], as[3];
#pragma unroll
      for (int k = 0; k < 3; ++k) {
        const float m = __builtin_amdgcn_exp2f(p[0] * hh[k]), ph = __builtin_amdgcn_fractf(p[1] * hh[k]);
        ac[k] = m * __builtin_amdgcn_cosf(ph);
        as[k] = -m * __builtin_amdgcn_sinf(ph);
      }
#pragma unroll
      for (int k = 0; k < 3; ++k) acc[k] = __builtin_amdgcn_mfma_f32_16x16x4f32(ac[k], br, acc[k], 0, 0, 0);
#pragma unroll
      for (int k = 0; k < 3; ++k) acc[k] = __builtin_amdgcn_mfma_f32_16x16x4f32(as[k], bi, acc[k], 0, 0, 0);
    }
#pragma unroll
    for (int k = 0; k < 3; ++k) red[(wv * 3 + k) * 64 + lane] = acc[k];
  }
  __syncthreads();
  if (tid < 192) {
    const int k = wv;
    const f32x4 s = (red[k * 64 + lane] + red[(3 + k) * 64 + lane]) + (red[(6 + k) * 64 + lane] + red[(9 + k) * 64 + lane]);
#pragma unroll
    for (int i = 0; i < 4; ++i) {
      const int r = 4 * gq + i;
      int hp;
      if (k == 0) hp = 1 + (r & 7) + 128 * (r >> 3);
      else if (k == 1) hp = (r < 8) ? 258 + 2 * (r & 3) + 128 * ((r >> 2) & 1) : 516 + 4 * (r & 1) + 128 * ((r - 8) >> 1);
      else hp = (r < 8) ? 1032 + 128 * r : 0;
      const int p = hp + 8 * c;
      if (k < 2 || r < 8 || (r == 8 && c == 0)) T[p] = s[i];
    }
  }
  __syncthreads();
  {
    const int j = tid >> 2, q = tid & 3;
    u32x4 hi, lo;
#pragma unroll
    for (int e = 0; e < 4; ++e) {
      const int m0 = 32 * j + 31 - (8 * q + 2 * e);
      const float k0 = T[(int)rbf((float)m0)], k1 = T[(int)rbf((float)(m0 - 1))];
      const float a0 = rbf(k0), a1 = rbf(k1);
      hi[e] = pk2(a0, a1);
      lo[e] = pk2(k0 - a0, k1 - a1);
    }
    char* kt = ws + OFF_KT + (size_t)d * 8192 + j * 128 + q * 16;
    *(u32x4*)kt = hi;
    *(u32x4*)(kt + 64) = lo;
  }
}

__global__ __launch_bounds__(256) void k1_pre(const u16* __restrict__ x, const u16* __restrict__ gw, const u16* __restrict__ lar,
                                              const u16* __restrict__ aim, const u16* __restrict__ cr, const u16* __restrict__ ci,
                                              const u16* __restrict__ ldt, const u16* __restrict__ w1,
                                              const u16* __restrict__ w2, char* __restrict__ ws) {
  __shared__ __attribute__((aligned(16))) char lds[KG_LDS];
  static_assert(KG_LDS >= 16 * LN_PITCH, "lds");
  const int b = blockIdx.x;
  if (b < 256) {
    __builtin_amdgcn_s_setprio(3);
    if (b == 0) for (int i = threadIdx.x; i < CTL_WORDS; i += 256) ((u32*)(ws + OFF_CTL))[i] = 0u;
    ln_block(b, x, gw, ws, lds);
    {
      const int tid = threadIdx.x, xg = b & 7, j = b >> 3;
      u32 acc = 0;
      const __amdgpu_buffer_rsrc_t rs1 = mk_rsrc(w1, (unsigned)((size_t)L_ * F_ * 2));
      for (int id = tid; id < 64 * 9; id += 256) {
        const int r = (id / 9) * 32 + j, ch = id % 9;
        acc ^= __builtin_amdgcn_raw_buffer_load_b32(rs1, (r * F_ + xg * 576 + ch * 64) * 2, 0, PF_AUX);
      }
      asm volatile("" ::"v"(acc));
    }
  } else {
    kgen_block(b - 256, lar, aim, cr, ci, ldt, ws, lds);
  }
}

constexpr int UC_STRIDE = 4160, KR_OFF = 8 * UC_STRIDE, KR_STRIDE = 160, CONV_LDS = KR_OFF + 80 * KR_STRIDE;
struct ConvIn { u32x4 c0, c1, k0, k1; u32 dk; };
DI void conv_load(ConvIn& in, int d0, const u16* __restrict__ dsk, const char* __restrict__ ws) {
  const int half = threadIdx.x >> 8, tid = threadIdx.x & 255;
  const int d = d0 + half;
  const char* xf = ws + OFF_XF + (size_t)(d >> 4) * 65536 + (d & 15) * 16;
  const u32x4* kt = (const u32x4*)(ws + OFF_KT + (size_t)d * 8192);
  const u32x4 z4 = {0, 0, 0, 0};
  in.c0 = *(const u32x4*)(xf + tid * 256);
  in.c1 = (tid < 255) ? *(const u32x4*)(xf + (tid + 1) * 256) : z4;
  in.k0 = kt[tid]; in.k1 = kt[tid + 256];
  in.dk = dsk[d];
}
DI void conv_pair(const ConvIn& in, int d0, char* __restrict__ ws, char* lds0) {
  const int half = threadIdx.x >> 8, tid = threadIdx.x & 255, lane = tid & 63, wv = tid >> 6, gq = lane >> 4, c = lane & 15;
  const int d = d0 + half;
  TS(ws, 11);
  char* lds = lds0 + half * CONV_LDS;
  const u32x4 z4 = {0, 0, 0, 0};
  *(u32x4*)(lds + KR_OFF + (16 + (tid >> 3)) * KR_STRIDE + (tid & 7) * 16) = in.k0;
  *(u32x4*)(lds + KR_OFF + (16 + 32 + (tid >> 3)) * KR_STRIDE + (tid & 7) * 16) = in.k1;
  if (tid < 128) *(u32x4*)(lds + KR_OFF + (tid >> 3) * KR_STRIDE + (tid & 7) * 16) = z4;
  auto put = [&](u32x4 a, u32x4 b, int X) {
    u32 e[8] = {a[0], a[1], a[2], a[3], b[0], b[1], b[2], b[3]};
#pragma unroll
    for (int cp = 0; cp < 8; ++cp) {
      const int sh = cp + 1;
      u32x4 o;
#pragma unroll
      for (int j = 0; j < 4; ++j) {
        if (sh & 1) o[j] = __builtin_amdgcn_alignbyte(e[(sh >> 1) + j + 1], e[(sh >> 1) + j], 2);
        else o[j] = (sh == 8) ? e[4 + j] : e[(sh >> 1) + j];
      }
      *(u32x4*)(lds + cp * UC_STRIDE + X * 16) = o;
    }
  };
  TS(ws, 12);
  put(in.c0, in.c1, tid + 4);
  if (tid < 4) put(z4, (tid == 0) ? in.c0 : z4, 3 - tid);
  __syncthreads();
  TS(ws, 13);
  f32x4 acc[4][2];
#pragma unroll
  for (int a = 0; a < 4; ++a) { acc[a][0] = f32x4{0, 0, 0, 0}; acc[a][1] = f32x4{0, 0, 0, 0}; }
  const char* abase = lds + (c & 7) * UC_STRIDE + (8 * gq + (c & 8)) * 2 + wv * 256;
  const char* bbase = lds + KR_OFF + (16 + c - 4 * wv) * KR_STRIDE + gq * 16;
  bf16x8 A[2][4][2], Bh[2][4], Bl[2][4];
  auto ldf = [&](int it, int s) {
#pragma unroll
    for (int k = 0; k < 4; ++k)
#pragma unroll
      for (int rt = 0; rt < 2; ++rt) A[s][k][rt] = *(const bf16x8*)(abase + (32 * (it + 16 * k) + 16 * rt) * 2);
#pragma unroll
    for (int m = 0; m < 4; ++m) {
      Bh[s][m] = *(const bf16x8*)(bbase + (16 * m - it) * KR_STRIDE);
      Bl[s][m] = *(const bf16x8*)(bbase + (16 * m - it) * KR_STRIDE + 64);
    }
  };
  ldf(0, 0);
#pragma unroll
  for (int it = 0; it < 4; ++it) {
    if (it + 1 < 4) ldf(it + 1, (it + 1) & 1);
#pragma unroll
    for (int k = 0; k < 4; ++k)
#pragma unroll
      for (int ct = k; ct < 4; ++ct)
#pragma unroll
        for (int rt = 0; rt < 2; ++rt) {
          acc[ct][rt] = mfma_bf(A[it & 1][k][rt], Bh[it & 1][ct - k], acc[ct][rt]);
          acc[ct][rt] = mfma_bf(A[it & 1][k][rt], Bl[it & 1][ct - k], acc[ct][rt]);
        }
  }
  TS(ws, 14);
  __syncthreads();
  f32x4* red = (f32x4*)lds;
  float* ylin = (float*)(lds + 32768);
#pragma unroll
  for (int ct = 0; ct < 4; ++ct)
#pragma unroll
    for (int rt = 0; rt < 2; ++rt) red[(wv * 8 + ct * 2 + rt) * 64 + lane] = acc[ct][rt];
  __syncthreads();
#pragma unroll
  for (int h = 0; h < 2; ++h) {
    const int fr = wv * 2 + h;
    const int ct = fr >> 1, rt = fr & 1;
    f32x4 s = red[(0 * 8 + fr) * 64 + lane] + red[(1 * 8 + fr) * 64 + lane] + red[(2 * 8 + fr) * 64 + lane] + red[(3 * 8 + fr) * 64 + lane];
    const int i = 16 * ct + c;
    *(f32x4*)(ylin + 36 * i + 16 * rt + 4 * gq) = s;
  }
  __syncthreads();
  {
    const float dskv = bf2f((u16)in.dk);
    const float* yp = ylin + 36 * (tid >> 2) + 8 * (tid & 3);
    const f32x4 y0 = *(const f32x4*)yp, y1 = *(const f32x4*)(yp + 4);
    u32x4 o;
    o[0] = pk2(rbf(y0[0]) + rbf(bflo(in.c0[0]) * dskv), rbf(y0[1]) + rbf(bfhi(in.c0[0]) * dskv));
    o[1] = pk2(rbf(y0[2]) + rbf(bflo(in.c0[1]) * dskv), rbf(y0[3]) + rbf(bfhi(in.c0[1]) * dskv));
    o[2] = pk2(rbf(y1[0]) + rbf(bflo(in.c0[2]) * dskv), rbf(y1[1]) + rbf(bfhi(in.c0[2]) * dskv));
    o[3] = pk2(rbf(y1[2]) + rbf(bflo(in.c0[3]) * dskv), rbf(y1[3]) + rbf(bfhi(in.c0[3]) * dskv));
    const __amdgpu_buffer_rsrc_t ars = mk_rsrc(ws + OFF_AT, (unsigned)((size_t)D_ * L_ * 2));
    __builtin_amdgcn_raw_buffer_store_b128(o, ars, d * 4096 + tid * 16, 0, 16);
  }
  TS(ws, 15);
}

DI float gelu_t(float u) {
  const float p = fmaf(u * u, 0.10294324f, 2.3022082f);
  const float e = __builtin_amdgcn_exp2f(u * p);
  return fmaf(-u, __builtin_amdgcn_rcpf(e + 1.f), u);
}
template <int OFF2>
DI bf16x8 tr_frag(const char* ad) {
  s16x4 lo = __builtin_amdgcn_ds_read_tr16_b64_v4i16((LAS s16x4*)(ad));
  s16x4 hi = __builtin_amdgcn_ds_read_tr16_b64_v4i16((LAS s16x4*)(ad + OFF2));
  return __builtin_shufflevector(lo, hi, 0, 1, 2, 3, 4, 5, 6, 7);
}

DI void gemm1_block(int bid, const u16* __restrict__ w1, const u16* __restrict__ b1, char* __restrict__ ws, char* lds,
                    ConvIn& cin, int cd0, const u16* __restrict__ dsk, const u16* __restrict__ w2) {
  constexpr int R = G1_R;
  constexpr int WD = 4;
  constexpr int NST = 16;
  TS(ws, 0);
  const int xcd = bid & 7, loc = bid >> 3;
  const int ft = xcd * 9 + loc / 3, dg = loc % 3;
  const int tid = threadIdx.x, lane = tid & 63, gq = lane >> 4, q = (lane & 15) >> 2, p = lane & 3;
  const int wv = __builtin_amdgcn_readfirstlane(tid >> 6), dgp = wv & 3, kh = wv >> 2;
  const int dt0 = dg * 12 + dgp * 3;
  const int srow = tid >> 3, sch = tid & 7;
  const int ssw = ((srow >> 1) & 1) | (((srow >> 3) & 1) << 1);
  const int st_off = srow * 128 + ((sch ^ (ssw << 1)) * 16);
  const __amdgpu_buffer_rsrc_t wrs = mk_rsrc(w1 + ft * 64, 0x7fffffff);
  const int woff = (srow * F_ + sch * 8) * 2;
  constexpr int wst = 128 * F_ * 2, whalf = 64 * F_ * 2;
  const int fsw = (q >> 1) | ((gq & 1) << 1);
  const int rd_base = kh * 4096 + (8 * gq + q) * 128 + 8 * p;
  const char* frl = ws + OFF_HF + (size_t)dt0 * 64 * 1024 + kh * 1024 + lane * 16;
  const u32x2 ba = *(const u32x2*)(b1 + ft * 64 + 32 * kh + 4 * gq);
  const u32x2 bb = *(const u32x2*)(b1 + ft * 64 + 32 * kh + 16 + 4 * gq);
  f32x4 acc[4][3];
#pragma unroll
  for (int a = 0; a < 4; ++a)
#pragma unroll
    for (int b = 0; b < 3; ++b) acc[a][b] = f32x4{0, 0, 0, 0};
  u32x4 wr[WD][2];
  bf16x8 hb[R][3];
  bf16x8 wf[2][4];
  u32 pf[2] = {0, 0};
  const __amdgpu_buffer_rsrc_t w2rs = mk_rsrc(w2, (unsigned)((size_t)L_ * F_ * 2));
  auto ldw = [&](int T, int h) -> u32x4 { return __builtin_amdgcn_raw_buffer_load_b128(wrs, woff + T * wst + h * whalf, 0, W1_AUX); };
  auto ldh = [&](int i, int b) -> bf16x8 { return *(const bf16x8*)(frl + (size_t)b * 64 * 1024 + (size_t)i * 2048); };
  auto rdw = [&](int i, int a) -> bf16x8 { return tr_frag<512>(lds + ((i >> 1) % 3) * 16384 + (i & 1) * 8192 + rd_base + ((a ^ fsw) * 32)); };
#pragma unroll
  for (int i = 0; i < WD; ++i) { wr[i][0] = ldw(i, 0); wr[i][1] = ldw(i, 1); }
#pragma unroll
  for (int j = 0; j < R - 1; ++j)
#pragma unroll
    for (int b = 0; b < 3; ++b) hb[j][b] = ldh(j, b);
#pragma unroll
  for (int i = 0; i < 2; ++i) {
    *(u32x4*)(lds + i * 16384 + st_off) = wr[i][0];
    *(u32x4*)(lds + i * 16384 + 8192 + st_off) = wr[i][1];
    wr[i][0] = ldw(i + WD, 0); wr[i][1] = ldw(i + WD, 1);
  }
  __syncthreads();
  TS(ws, 1);
#pragma unroll
  for (int a = 0; a < 4; ++a) wf[0][a] = rdw(0, a);
#pragma unroll
  for (int i = 0; i < 32; ++i) {
    const int T = i >> 1, h = i & 1;
#pragma unroll
    for (int a = 0; a < 4; ++a) {
      SB();
#pragma unroll
      for (int b = 0; b < 3; ++b) acc[a][b] = mfma_bf(wf[i & 1][a], hb[i % R][b], acc[a][b]);
      if (i + 1 < 32) wf[(i + 1) & 1][a] = rdw(i + 1, a);
      if (a < 3) { if (i + R - 1 < 32) hb[(i + R - 1) % R][a] = ldh(i + R - 1, a); }
      if (a == 3) {
        if (T + 2 < NST) {
          *(u32x4*)(lds + ((T + 2) % 3) * 16384 + h * 8192 + st_off) = wr[(T + 2) % WD][h];
          if (T + 2 + WD < NST) wr[(T + 2) % WD][h] = ldw(T + 2 + WD, h);
        }
        if (i == PF2_START || i == PF2_START + 4) {
          const int k = (i - PF2_START) / 4, id = tid + 512 * k, r = 171 * loc + (id >> 2);
          const int off = ((id >> 2) < 171 && r < F_) ? (r * L_ + xcd * 256 + (id & 3) * 64) * 2 : 0x7ffffff0;
          pf[k] = __builtin_amdgcn_raw_buffer_load_b32(w2rs, off, 0, PF_AUX);
        }
      }
    }
    SB();
    if (h == 1) __syncthreads();
  }
  asm volatile("" ::"v"(pf[0] ^ pf[1]));
  TS(ws, 2);
  if (cd0 >= 0) conv_load(cin, cd0, dsk, ws);
  f32x4* red = (f32x4*)lds;
#pragma unroll
  for (int a2 = 0; a2 < 2; ++a2)
#pragma unroll
    for (int b = 0; b < 3; ++b) red[((wv * 2 + a2) * 3 + b) * 64 + lane] = kh ? acc[a2][b] : acc[2 + a2][b];
  __syncthreads();
  const float bia[4] = {bflo(ba[0]), bfhi(ba[0]), bflo(ba[1]), bfhi(ba[1])};
  const float bib[4] = {bflo(bb[0]), bfhi(bb[0]), bflo(bb[1]), bfhi(bb[1])};
  const __amdgpu_buffer_rsrc_t hrs = mk_rsrc(ws + OFF_HH, (unsigned)((size_t)36 * 144 * 1024));
#pragma unroll
  for (int b = 0; b < 3; ++b) {
    const f32x4 oa = red[(((wv ^ 4) * 2 + 0) * 3 + b) * 64 + lane], ob = red[(((wv ^ 4) * 2 + 1) * 3 + b) * 64 + lane];
    const f32x4 sa = (kh ? acc[2][b] : acc[0][b]) + oa, sb = (kh ? acc[3][b] : acc[1][b]) + ob;
    float ra[4], rb[4];
#pragma unroll
    for (int i = 0; i < 4; ++i) {
      ra[i] = gelu_t(rbf(rbf(sa[i]) + bia[i]));
      rb[i] = gelu_t(rbf(rbf(sb[i]) + bib[i]));
    }
    u32x4 o = {pk2(ra[0], ra[1]), pk2(ra[2], ra[3]), pk2(rb[0], rb[1]), pk2(rb[2], rb[3])};
    __builtin_amdgcn_raw_buffer_store_b128(o, hrs, (((dt0 + b) * 144 + ft * 2 + kh) * 64 + lane) * 16, 0, 16);
  }
  TS(ws, 3);
}

DI void publish(gu32* ctr) {
  asm volatile("s_waitcnt vmcnt(0)" ::: "memory");
  __syncthreads();
  if (threadIdx.x < NREP) __hip_atomic_fetch_add(ctr + 64 * threadIdx.x, 1u, RLX_AGENT);
}
DI void wait_ge(gu32* word, unsigned want, gu32* tmo) {
  if (threadIdx.x == 0) {
    for (unsigned spins = 0;;) {
      if (__hip_atomic_load(word, RLX_AGENT) >= want) break;
      __builtin_amdgcn_s_sleep(1);
      if (++spins > (1u << 20)) { __hip_atomic_store(tmo, 1u, RLX_AGENT); break; }
      if ((spins & 1023u) == 0u && __hip_atomic_load(tmo, RLX_AGENT) != 0u) break;
    }
    __builtin_amdgcn_fence(__ATOMIC_ACQUIRE, "wavefront");
  }
  __syncthreads();
}

DI void gemm2_block(int st, int dgi, const u16* __restrict__ w2, const u16* __restrict__ b2, char* __restrict__ ws,
                    u16* __restrict__ out, char* lds) {
  constexpr int RW = 2, RH = 3, NS = 18, NDT = 5;
  gu32* ctl = (gu32*)(ws + OFF_CTL);
  const int tid = threadIdx.x, lane = tid & 63, gq = lane >> 4, q = (lane & 15) >> 2, p = lane & 3, c = lane & 15;
  const int wv = __builtin_amdgcn_readfirstlane(tid >> 6);
  const int d0 = dgi * 72;
  const int dtb = d0 >> 4;
  const bool odd = dgi & 1;
  char* my = lds + wv * 8192;
  const int srow = lane >> 3, sch = lane & 7;
  const __amdgpu_buffer_rsrc_t wrs = mk_rsrc(w2 + st * 64, 0x7fffffff);
  const int woff = ((wv * 32 + srow) * L_ + sch * 8) * 2;
  constexpr int wstep = 256 * L_ * 2, wrow8 = 8 * L_ * 2;
  const int st_off = srow * 128 + ((sch ^ (((srow >> 1) & 3) << 1)) * 16);
  const int fsw = (q >> 1) | ((gq & 1) << 1);
  const int rd_base = (4 * gq + q) * 128 + 8 * p;
  const __amdgpu_buffer_rsrc_t hrs = mk_rsrc(ws + OFF_HH, (unsigned)((size_t)36 * 144 * 1024));
  const int hoff = (dtb * 144 + wv) * 1024 + lane * 16;
  const int inv0 = (odd && c < 8) ? 0x40000000 : 0;
  const int inv4 = (!odd && c >= 8) ? 0x40000000 : 0;
  f32x4 acc[4][NDT];
#pragma unroll
  for (int a = 0; a < 4; ++a)
#pragma unroll
    for (int b = 0; b < NDT; ++b) acc[a][b] = f32x4{0, 0, 0, 0};
  u32x4 wr[RW][4];
  bf16x8 hf[RH][NDT];
  bf16x8 wf[2][4];
  auto ldw = [&](int i, int r) -> u32x4 { return __builtin_amdgcn_raw_buffer_load_b128(wrs, woff + i * wstep + r * wrow8, 0, W2_AUX); };
  auto ldh = [&](int i, int b) -> bf16x8 {
    const int o = hoff + b * 144 * 1024 + i * 8192 + (b == 0 ? inv0 : (b == 4 ? inv4 : 0));
    return __builtin_bit_cast(bf16x8, __builtin_amdgcn_raw_buffer_load_b128(hrs, o, 0, 16));
  };
  auto stw = [&](int i, int r, u32x4 v) { *(u32x4*)(my + (i & 1) * 4096 + r * 1024 + st_off) = v; };
  auto rdw = [&](int i, int a) -> bf16x8 { return tr_frag<2048>(my + (i & 1) * 4096 + rd_base + ((a ^ fsw) * 32)); };
#pragma unroll
  for (int i = 0; i < RW; ++i)
#pragma unroll
    for (int r = 0; r < 4; ++r) wr[i][r] = ldw(i, r);
  float* sg = (float*)(lds + 92416);
  if (tid < 64) sg[tid] = ((const float*)(ws + OFF_G))[st * 64 + tid];
  else if (tid < 128) sg[tid] = bf2f(b2[st * 64 + tid - 64]);
  TS(ws, 6);
  wait_ge(ctl + W_HFC + 64 * (blockIdx.x % NREP), 216u, ctl + W_TMO);
  TS(ws, 7);
#pragma unroll
  for (int i = 0; i < RH; ++i)
#pragma unroll
    for (int b = 0; b < NDT; ++b) hf[i][b] = ldh(i, b);
#pragma unroll
  for (int r = 0; r < 4; ++r) { stw(0, r, wr[0][r]); wr[0][r] = ldw(RW, r); }
#pragma unroll
  for (int a = 0; a < 4; ++a) wf[0][a] = rdw(0, a);
  TS(ws, 8);
#pragma unroll
  for (int i = 0; i < NS; ++i) {
    if (i + 1 < NS) {
      SB();
#pragma unroll
      for (int r = 0; r < 4; ++r) {
        stw(i + 1, r, wr[(i + 1) % RW][r]);
        if (i + 1 + RW < NS) wr[(i + 1) % RW][r] = ldw(i + 1 + RW, r);
      }
    }
#pragma unroll
    for (int a = 0; a < 4; ++a) {
      SB();
#pragma unroll
      for (int b = 0; b < NDT; ++b) acc[a][b] = mfma_bf(hf[i % RH][b], wf[i & 1][a], acc[a][b]);
      if (i + 1 < NS) wf[(i + 1) & 1][a] = rdw(i + 1, a);
    }
    SB();
    if (i + RH < NS) {
#pragma unroll
      for (int b = 0; b < NDT; ++b) hf[i % RH][b] = ldh(i + RH, b);
    }
  }
  TS(ws, 9);
  wait_ge(ctl + W_AC + 64 * (blockIdx.x % NREP), 288u, ctl + W_TMO);
  char* as = lds + 81920;
  const __amdgpu_buffer_rsrc_t ars = mk_rsrc(ws + OFF_AT, (unsigned)((size_t)D_ * L_ * 2));
  const u32x4 av0 = __builtin_amdgcn_raw_buffer_load_b128(ars, (d0 + (tid >> 3)) * 4096 + st * 128 + (tid & 7) * 16, 0, 16);
  u32x4 av1 = {0, 0, 0, 0};
  if (tid < 64) av1 = __builtin_amdgcn_raw_buffer_load_b128(ars, (d0 + 64 + (tid >> 3)) * 4096 + st * 128 + (tid & 7) * 16, 0, 16);
  f32x4* red = (f32x4*)lds;
#pragma unroll
  for (int rd = 0; rd < 2; ++rd) {
    if (rd) __syncthreads();
#pragma unroll
    for (int a2 = 0; a2 < 2; ++a2)
#pragma unroll
      for (int b = 0; b < NDT; ++b) red[(wv * (2 * NDT) + a2 * NDT + b) * 64 + lane] = acc[2 * rd + a2][b];
    if (rd == 0) {
      *(u32x4*)(as + (tid >> 3) * 144 + (tid & 7) * 16) = av0;
      if (tid < 64) *(u32x4*)(as + (64 + (tid >> 3)) * 144 + (tid & 7) * 16) = av1;
    }
    __syncthreads();
    for (int T = wv; T < 2 * NDT; T += 8) {
      f32x4 s = red[(0 * (2 * NDT) + T) * 64 + lane];
#pragma unroll
      for (int w = 1; w < 8; ++w) s += red[(w * (2 * NDT) + T) * 64 + lane];
      const int a = 2 * rd + T / NDT, b = T % NDT;
      const int sl = 16 * a + c, sidx = st * 64 + sl;
      const int dl = 16 * (dtb + b) + 4 * gq - d0;
      if (dl >= 0 && dl < 72) {
        const float gg = sg[sl], og = rbf(1.f - gg), bb = sg[64 + sl];
        float o[4];
#pragma unroll
        for (int i = 0; i < 4; ++i) {
          const float av = bf2f(*(const u16*)(as + (dl + i) * 144 + sl * 2));
          const float bv = rbf(rbf(s[i]) + bb);
          o[i] = rbf(gg * av) + rbf(og * bv);
        }
        u32x2 ov = {pk2(o[0], o[1]), pk2(o[2], o[3])};
        *(u32x2*)(out + (size_t)sidx * D_ + d0 + dl) = ov;
      }
    }
  }
  TS(ws, 10);
}

constexpr int K2_LDS = 2 * CONV_LDS;
__global__ __launch_bounds__(512) void k2_main(const u16* __restrict__ w1, const u16* __restrict__ b1, const u16* __restrict__ w2,
                                               const u16* __restrict__ b2, const u16* __restrict__ dsk, char* __restrict__ ws,
                                               u16* __restrict__ out) {
  __shared__ __attribute__((aligned(16))) char lds[98304];
  const int bid = blockIdx.x;
  gu32* ctl = (gu32*)(ws + OFF_CTL);
  if (bid < 216) {
    ConvIn cin;
    gemm1_block(bid, w1, b1, ws, lds, cin, bid < 168 ? 2 * (120 + bid) : -1, dsk, w2);
    publish(ctl + W_HFC);
    TS(ws, 4);

    if (bid < 168) { conv_pair(cin, 2 * (120 + bid), ws, lds); publish(ctl + W_AC); }
  } else {
    ConvIn ca, cb;
    const int p0 = (bid - 216) * 3;
    conv_load(ca, 2 * p0, dsk, ws);
    conv_load(cb, 2 * (p0 + 1), dsk, ws);
    conv_pair(ca, 2 * p0, ws, lds); __syncthreads();
    conv_load(ca, 2 * (p0 + 2), dsk, ws);
    conv_pair(cb, 2 * (p0 + 1), ws, lds); __syncthreads();
    conv_pair(ca, 2 * (p0 + 2), ws, lds);
    asm volatile("s_waitcnt vmcnt(0)" ::: "memory");
    __syncthreads();
    if (threadIdx.x < NREP) __hip_atomic_fetch_add(ctl + W_AC + 64 * threadIdx.x, 3u, RLX_AGENT);
  }
  TS(ws, 5);
  const int xcd = bid & 7, loc = bid >> 3;
  gemm2_block(xcd * 4 + (loc >> 3), loc & 7, w2, b2, ws, out, lds);
}

extern "C" void kernel_launch(void* const* d_in, const int* in_sizes, int n_in, void* d_out, int out_size, void* d_ws,
                              size_t ws_size, hipStream_t stream) {
  const u16* x = (const u16*)d_in[0];
  const u16* lar = (const u16*)d_in[1];
  const u16* aim = (const u16*)d_in[2];
  const u16* cr = (const u16*)d_in[3];
  const u16* ci = (const u16*)d_in[4];
  const u16* ldt = (const u16*)d_in[5];
  const u16* dsk = (const u16*)d_in[6];
  const u16* w1 = (const u16*)d_in[8];
  const u16* b1 = (const u16*)d_in[9];
  const u16* w2 = (const u16*)d_in[10];
  const u16* b2 = (const u16*)d_in[11];
  const u16* gw = (const u16*)d_in[12];
  char* ws = (char*)d_ws;
  k1_pre<<<256 + 576, 256, 0, stream>>>(x, gw, lar, aim, cr, ci, ldt, w1, w2, ws);
    k2_main<<<256, 512, 0, stream>>>(w1, b1, w2, b2, dsk, ws, (u16*)d_out);
}
```

```cpp
#include <hip/hip_runtime.h>
#include <stdint.h>

typedef unsigned short u16;
typedef unsigned int u32;
typedef short bf16x8 __attribute__((ext_vector_type(8)));
typedef short s16x4 __attribute__((ext_vector_type(4)));
typedef float f32x4 __attribute__((ext_vector_type(4)));
typedef unsigned u32x4 __attribute__((ext_vector_type(4)));
typedef unsigned u32x2 __attribute__((ext_vector_type(2)));
typedef __bf16 bf16x2_t __attribute__((ext_vector_type(2)));
#define LAS __attribute__((address_space(3)))
#define GAS __attribute__((address_space(1)))
#define DI __device__ __forceinline__
typedef GAS unsigned gu32;
#define RLX_AGENT __ATOMIC_RELAXED, __HIP_MEMORY_SCOPE_AGENT

constexpr int L_ = 2048, D_ = 576, N_ = 128, F_ = 4608;
constexpr size_t OFF_CTL = 0;
constexpr size_t OFF_HF = 65536;
constexpr size_t OFF_XF = OFF_HF + (size_t)36 * 64 * 1024;
constexpr size_t OFF_G = OFF_XF + (size_t)36 * 64 * 1024;
constexpr size_t OFF_KT = OFF_G + 8192;
constexpr size_t OFF_HH = OFF_KT + (size_t)D_ * 8192;
constexpr size_t OFF_AT = OFF_HH + (size_t)36 * 144 * 1024;
constexpr size_t WS_USED = OFF_AT + (size_t)D_ * L_ * 2;
constexpr int NREP = 16;
constexpr int W_HFC = 0;
constexpr int W_AC = 64 * NREP;
constexpr int W_TMO = 64 * 2 * NREP;
constexpr int CTL_WORDS = 64 * 2 * NREP + 64;

#define G1_R 7
#define G2_RW 2
#define G2_RH 4
#define W1_AUX 0
#define W2_AUX 0
#define CONV_LD_AT 27
#define NO_PF2
#define G2_RH8 4
#define G2_LD 4
#define POLL_SLEEP 4
#define PF2_ROWS 1024
#define PF_AUX 0
#define PF2_START 8
#define W_AUX 2
#define SB() __builtin_amdgcn_sched_barrier(0)
#define LBAR() do { asm volatile("s_waitcnt lgkmcnt(0)" ::: "memory"); __builtin_amdgcn_s_barrier(); asm volatile("" ::: "memory"); } while (0)

DI float bf2f(u16 b) { return __uint_as_float(((u32)b) << 16); }
DI float bflo(u32 w) { return __uint_as_float(w << 16); }
DI float bfhi(u32 w) { return __uint_as_float(w & 0xffff0000u); }
DI u32 pk2(float a, float b) { bf16x2_t v = {(__bf16)a, (__bf16)b}; return __builtin_bit_cast(u32, v); }
DI float rbf(float x) { return (float)(__bf16)x; }
DI f32x4 mfma_bf(bf16x8 a, bf16x8 b, f32x4 c) { return __builtin_amdgcn_mfma_f32_16x16x32_bf16(a, b, c, 0, 0, 0); }
DI __amdgpu_buffer_rsrc_t mk_rsrc(const void* p, unsigned bytes) { return __builtin_amdgcn_make_buffer_rsrc((void*)p, 0, bytes, 0x00020000); }
template <int CTRL, int RM>
DI float dpp_add(float v) {
  return v + __builtin_bit_cast(float, __builtin_amdgcn_update_dpp(0, __builtin_bit_cast(int, v), CTRL, RM, 0xf, false));
}
DI float wave_sum(float v) {
  v = dpp_add<0x111, 0xf>(v); v = dpp_add<0x112, 0xf>(v); v = dpp_add<0x114, 0xf>(v); v = dpp_add<0x118, 0xf>(v);
  v = dpp_add<0x142, 0xa>(v); v = dpp_add<0x143, 0xc>(v);
  return __builtin_bit_cast(float, __builtin_amdgcn_readlane(__builtin_bit_cast(int, v), 63));
}

constexpr int LN_PITCH = 1168;
DI void ln_block(int b, const u16* __restrict__ x, const u16* __restrict__ gw, const u16* __restrict__ w1, char* __restrict__ ws, char* lds) {
  const int tid = threadIdx.x, lane = tid & 63, wv = tid >> 6;
  const u32x4 z4 = {0, 0, 0, 0};
  const u32x4* gr = (const u32x4*)gw;
  u32x4 v[2][2], w[2];
#pragma unroll
  for (int r = 0; r < 2; ++r) {
    const u32x4* xr = (const u32x4*)(x + (size_t)(8 * b + 2 * wv + r) * D_);
    v[r][0] = xr[lane];
    v[r][1] = (lane < 8) ? xr[64 + lane] : z4;
  }
  w[0] = gr[lane]; w[1] = (lane < 8) ? gr[64 + lane] : z4;
  u32 pf[3] = {0, 0, 0};
  {
    asm volatile("" ::: "memory");
    const int xg = b & 7, j = b >> 3;
    const __amdgpu_buffer_rsrc_t rs1 = mk_rsrc(w1, (unsigned)((size_t)L_ * F_ * 2));
#pragma unroll
    for (int i = 0; i < 3; ++i) {
      const int id = tid + 256 * i, r = (id / 9) * 32 + j, ch = id % 9;
      pf[i] = __builtin_amdgcn_raw_buffer_load_b32(rs1, id < 576 ? (r * F_ + xg * 576 + ch * 64) * 2 : 0x7ffffff0, 0, PF_AUX);
    }
    asm volatile("" ::: "memory");
  }
#pragma unroll
  for (int r = 0; r < 2; ++r) {
    const int row = 8 * b + 2 * wv + r;
    float sum = 0.f, gs = 0.f;
#pragma unroll
    for (int i = 0; i < 2; ++i)
#pragma unroll
      for (int j = 0; j < 4; ++j) {
        float a = bflo(v[r][i][j]), c = bfhi(v[r][i][j]);
        sum += a + c;
        gs += rbf(a * bflo(w[i][j])) + rbf(c * bfhi(w[i][j]));
      }
    sum = wave_sum(sum); gs = wave_sum(gs);
    const float mean = sum * (1.f / D_);
    float vs = 0.f;
#pragma unroll
    for (int j = 0; j < 4; ++j) {
      float a = bflo(v[r][0][j]) - mean, c = bfhi(v[r][0][j]) - mean;
      vs += a * a + c * c;
    }
    if (lane < 8) {
#pragma unroll
      for (int j = 0; j < 4; ++j) {
        float a = bflo(v[r][1][j]) - mean, c = bfhi(v[r][1][j]) - mean;
        vs += a * a + c * c;
      }
    }
    vs = wave_sum(vs);
    const float var = rbf(vs * (1.f / D_)), m = rbf(mean);
    const float den = rbf(sqrtf(rbf(var + 1e-5f)));
    const float rden = 1.f / den;
    if (lane == 0) {
      float z = rbf(gs);
      ((float*)(ws + OFF_G))[row] = rbf(1.f / (1.f + __expf(-z)));
    }
#pragma unroll
    for (int i = 0; i < 2; ++i) {
      if (i == 0 || lane < 8) {
        u32x4 o;
#pragma unroll
        for (int j = 0; j < 4; ++j) o[j] = pk2(rbf(bflo(v[r][i][j]) - m) * rden, rbf(bfhi(v[r][i][j]) - m) * rden);
        *(u32x4*)(lds + (2 * wv + r) * LN_PITCH + (64 * i + lane) * 16) = o;
        *(u32x4*)(lds + (8 + 2 * wv + r) * LN_PITCH + (64 * i + lane) * 16) = v[r][i];
      }
    }
  }
  LBAR();
  for (int pc = tid; pc < 2 * D_; pc += 256) {
    const int which = pc >= D_, d = which ? pc - D_ : pc;
    const char* src = lds + which * 8 * LN_PITCH + d * 2;
    u16 e[8];
#pragma unroll
    for (int r = 0; r < 8; ++r) e[r] = *(const u16*)(src + r * LN_PITCH);
    u32x4 o;
#pragma unroll
    for (int j = 0; j < 4; ++j) o[j] = (u32)e[2 * j] | ((u32)e[2 * j + 1] << 16);
    *(u32x4*)(ws + (which ? OFF_XF : OFF_HF) + (size_t)(d >> 4) * 65536 + b * 256 + (d & 15) * 16) = o;
  }
  asm volatile("" ::"v"(pf[0]), "v"(pf[1]), "v"(pf[2]));
}

constexpr int KG_LDS = 2048 + 12288 + 8208;
DI void kgen_block(int d, const u16* __restrict__ lar, const u16* __restrict__ aim, const u16* __restrict__ cr,
                   const u16* __restrict__ ci, const u16* __restrict__ ldt, char* __restrict__ ws, char* lds) {
  const int tid = threadIdx.x, lane = tid & 63, wv = tid >> 6, gq = lane >> 4, c = lane & 15;
  f32x4* par = (f32x4*)lds;
  f32x4* red = (f32x4*)(lds + 2048);
  float* T = (float*)(lds + 2048 + 12288);
  if (lane < 32) {
    const int n = d * N_ + 32 * wv + lane;
    const u16 vlar = lar[n], vaim = aim[n], vcr = cr[n], vci = ci[n], vldt = ldt[d];
    const float dt = rbf(__expf(bf2f(vldt)));
    f32x4 p;
    p[0] = -rbf(__expf(bf2f(vlar))) * dt * 1.4426950408889634f;
    p[1] = bf2f(vaim) * dt * 0.15915494309189535f;
    p[2] = 2.f * (bf2f(vcr) * dt);
    p[3] = 2.f * (bf2f(vci) * dt);
    par[32 * wv + lane] = p;
  }
  __builtin_amdgcn_fence(__ATOMIC_RELEASE, "wavefront");
  __builtin_amdgcn_wave_barrier();
  {
    float hh[3];
    hh[0] = (float)(1 + (c & 7) + 128 * (c >> 3));
    hh[1] = (c < 8) ? (float)(258 + 2 * (c & 3) + 128 * ((c >> 2) & 1)) : (float)(516 + 4 * (c & 1) + 128 * ((c - 8) >> 1));
    hh[2] = (c < 8) ? (float)(1032 + 128 * c) : 0.f;
    const float lc = (float)(8 * c);
    f32x4 acc[3] = {{0, 0, 0, 0}, {0, 0, 0, 0}, {0, 0, 0, 0}};
#pragma unroll 2
    for (int t = 0; t < 8; ++t) {
      const f32x4 p = par[32 * wv + 4 * t + gq];
      const float bm = __builtin_amdgcn_exp2f(p[0] * lc), bp = __builtin_amdgcn_fractf(p[1] * lc);
      const float er = bm * __builtin_amdgcn_cosf(bp), ei = bm * __builtin_amdgcn_sinf(bp);
      const float br = fmaf(p[2], er, -p[3] * ei), bi = fmaf(p[2], ei, p[3] * er);
      float ac[3], as[3];
#pragma unroll
      for (int k = 0; k < 3; ++k) {
        const float m = __builtin_amdgcn_exp2f(p[0] * hh[k]), ph = __builtin_amdgcn_fractf(p[1] * hh[k]);
        ac[k] = m * __builtin_amdgcn_cosf(ph);
        as[k] = -m * __builtin_amdgcn_sinf(ph);
      }
#pragma unroll
      for (int k = 0; k < 3; ++k) acc[k] = __builtin_amdgcn_mfma_f32_16x16x4f32(ac[k], br, acc[k], 0, 0, 0);
#pragma unroll
      for (int k = 0; k < 3; ++k) acc[k] = __builtin_amdgcn_mfma_f32_16x16x4f32(as[k], bi, acc[k], 0, 0, 0);
    }
#pragma unroll
    for (int k = 0; k < 3; ++k) red[(wv * 3 + k) * 64 + lane] = acc[k];
  }
  __syncthreads();
  if (tid < 192) {
    const int k = wv;
    const f32x4 s = (red[k * 64 + lane] + red[(3 + k) * 64 + lane]) + (red[(6 + k) * 64 + lane] + red[(9 + k) * 64 + lane]);
#pragma unroll
    for (int i = 0; i < 4; ++i) {
      const int r = 4 * gq + i;
      int hp;
      if (k == 0) hp = 1 + (r & 7) + 128 * (r >> 3);
      else if (k == 1) hp = (r < 8) ? 258 + 2 * (r & 3) + 128 * ((r >> 2) & 1) : 516 + 4 * (r & 1) + 128 * ((r - 8) >> 1);
      else hp = (r < 8) ? 1032 + 128 * r : 0;
      const int p = hp + 8 * c;
      if (k < 2 || r < 8 || (r == 8 && c == 0)) T[p] = s[i];
    }
  }
  __syncthreads();
  {
    const int j = tid >> 2, q = tid & 3;
    u32x4 hi, lo;
#pragma unroll
    for (int e = 0; e < 4; ++e) {
      const int m0 = 32 * j + 31 - (8 * q + 2 * e);
      const float k0 = T[(int)rbf((float)m0)], k1 = T[(int)rbf((float)(m0 - 1))];
      const float a0 = rbf(k0), a1 = rbf(k1);
      hi[e] = pk2(a0, a1);
      lo[e] = pk2(k0 - a0, k1 - a1);
    }
    char* kt = ws + OFF_KT + (size_t)d * 8192 + j * 128 + q * 16;
    *(u32x4*)kt = hi;
    *(u32x4*)(kt + 64) = lo;
  }
}

__global__ __launch_bounds__(256) void k1_pre(const u16* __restrict__ x, const u16* __restrict__ gw, const u16* __restrict__ lar,
                                              const u16* __restrict__ aim, const u16* __restrict__ cr, const u16* __restrict__ ci,
                                              const u16* __restrict__ ldt, const u16* __restrict__ w1,
                                              const u16* __restrict__ w2, char* __restrict__ ws) {
  __shared__ __attribute__((aligned(16))) char lds[KG_LDS];
  const int b = blockIdx.x;
  if (b < 256) {
    __builtin_amdgcn_s_setprio(3);
    if (b == 0) for (int i = threadIdx.x; i < CTL_WORDS; i += 256) ((u32*)(ws + OFF_CTL))[i] = 0u;
    ln_block(b, x, gw, w1, ws, lds);
  } else {
    kgen_block(b - 256, lar, aim, cr, ci, ldt, ws, lds);
  }
}

constexpr int UC_STRIDE = 4160, KR_OFF = 8 * UC_STRIDE, KR_STRIDE = 160, CONV_LDS = KR_OFF + 80 * KR_STRIDE;
struct ConvIn { u32x4 c0, c1, k0, k1; u32 dk; };
DI void conv_load(ConvIn& in, int d0, const u16* __restrict__ dsk, const char* __restrict__ ws) {
  const int half = threadIdx.x >> 8, tid = threadIdx.x & 255;
  const int d = d0 + half;
  const char* xf = ws + OFF_XF + (size_t)(d >> 4) * 65536 + (d & 15) * 16;
  const u32x4* kt = (const u32x4*)(ws + OFF_KT + (size_t)d * 8192);
  const u32x4 z4 = {0, 0, 0, 0};
  in.c0 = *(const u32x4*)(xf + tid * 256);
  in.c1 = (tid < 255) ? *(const u32x4*)(xf + (tid + 1) * 256) : z4;
  in.k0 = kt[tid]; in.k1 = kt[tid + 256];
  in.dk = dsk[d];
}
DI void conv_pair(const ConvIn& in, int d0, char* __restrict__ ws, char* lds0) {
  const int half = threadIdx.x >> 8, tid = threadIdx.x & 255, lane = tid & 63, wv = tid >> 6, gq = lane >> 4, c = lane & 15;
  const int d = d0 + half;
  char* lds = lds0 + half * CONV_LDS;
  const u32x4 z4 = {0, 0, 0, 0};
  *(u32x4*)(lds + KR_OFF + (16 + (tid >> 3)) * KR_STRIDE + (tid & 7) * 16) = in.k0;
  *(u32x4*)(lds + KR_OFF + (16 + 32 + (tid >> 3)) * KR_STRIDE + (tid & 7) * 16) = in.k1;
  if (tid < 128) *(u32x4*)(lds + KR_OFF + (tid >> 3) * KR_STRIDE + (tid & 7) * 16) = z4;
  auto put = [&](u32x4 a, u32x4 b, int X) {
    u32 e[8] = {a[0], a[1], a[2], a[3], b[0], b[1], b[2], b[3]};
#pragma unroll
    for (int cp = 0; cp < 8; ++cp) {
      const int sh = cp + 1;
      u32x4 o;
#pragma unroll
      for (int j = 0; j < 4; ++j) {
        if (sh & 1) o[j] = __builtin_amdgcn_alignbyte(e[(sh >> 1) + j + 1], e[(sh >> 1) + j], 2);
        else o[j] = (sh == 8) ? e[4 + j] : e[(sh >> 1) + j];
      }
      *(u32x4*)(lds + cp * UC_STRIDE + X * 16) = o;
    }
  };
  put(in.c0, in.c1, tid + 4);
  if (tid < 4) put(z4, (tid == 0) ? in.c0 : z4, 3 - tid);
  LBAR();
  f32x4 acc[4][2];
#pragma unroll
  for (int a = 0; a < 4; ++a) { acc[a][0] = f32x4{0, 0, 0, 0}; acc[a][1] = f32x4{0, 0, 0, 0}; }
  const char* abase = lds + (c & 7) * UC_STRIDE + (8 * gq + (c & 8)) * 2 + wv * 256;
  const char* bbase = lds + KR_OFF + (16 + c - 4 * wv) * KR_STRIDE + gq * 16;
  bf16x8 A[2][4][2], Bh[2][4], Bl[2][4];
  auto ldf = [&](int it, int s) {
#pragma unroll
    for (int k = 0; k < 4; ++k)
#pragma unroll
      for (int rt = 0; rt < 2; ++rt) A[s][k][rt] = *(const bf16x8*)(abase + (32 * (it + 16 * k) + 16 * rt) * 2);
#pragma unroll
    for (int m = 0; m < 4; ++m) {
      Bh[s][m] = *(const bf16x8*)(bbase + (16 * m - it) * KR_STRIDE);
      Bl[s][m] = *(const bf16x8*)(bbase + (16 * m - it) * KR_STRIDE + 64);
    }
  };
  ldf(0, 0);
#pragma unroll
  for (int it = 0; it < 4; ++it) {
    if (it + 1 < 4) ldf(it + 1, (it + 1) & 1);
#pragma unroll
    for (int k = 0; k < 4; ++k)
#pragma unroll
      for (int ct = k; ct < 4; ++ct)
#pragma unroll
        for (int rt = 0; rt < 2; ++rt) {
          acc[ct][rt] = mfma_bf(A[it & 1][k][rt], Bh[it & 1][ct - k], acc[ct][rt]);
          acc[ct][rt] = mfma_bf(A[it & 1][k][rt], Bl[it & 1][ct - k], acc[ct][rt]);
        }
  }
  LBAR();
  f32x4* red = (f32x4*)lds;
  float* ylin = (float*)(lds + 32768);
#pragma unroll
  for (int ct = 0; ct < 4; ++ct)
#pragma unroll
    for (int rt = 0; rt < 2; ++rt) red[(wv * 8 + ct * 2 + rt) * 64 + lane] = acc[ct][rt];
  LBAR();
#pragma unroll
  for (int h = 0; h < 2; ++h) {
    const int fr = wv * 2 + h;
    const int ct = fr >> 1, rt = fr & 1;
    f32x4 s = red[(0 * 8 + fr) * 64 + lane] + red[(1 * 8 + fr) * 64 + lane] + red[(2 * 8 + fr) * 64 + lane] + red[(3 * 8 + fr) * 64 + lane];
    const int i = 16 * ct + c;
    *(f32x4*)(ylin + 36 * i + 16 * rt + 4 * gq) = s;
  }
  LBAR();
  {
    const float dskv = bf2f((u16)in.dk);
    const float* yp = ylin + 36 * (tid >> 2) + 8 * (tid & 3);
    const f32x4 y0 = *(const f32x4*)yp, y1 = *(const f32x4*)(yp + 4);
    u32x4 o;
    o[0] = pk2(rbf(y0[0]) + rbf(bflo(in.c0[0]) * dskv), rbf(y0[1]) + rbf(bfhi(in.c0[0]) * dskv));
    o[1] = pk2(rbf(y0[2]) + rbf(bflo(in.c0[1]) * dskv), rbf(y0[3]) + rbf(bfhi(in.c0[1]) * dskv));
    o[2] = pk2(rbf(y1[0]) + rbf(bflo(in.c0[2]) * dskv), rbf(y1[1]) + rbf(bfhi(in.c0[2]) * dskv));
    o[3] = pk2(rbf(y1[2]) + rbf(bflo(in.c0[3]) * dskv), rbf(y1[3]) + rbf(bfhi(in.c0[3]) * dskv));
    const __amdgpu_buffer_rsrc_t ars = mk_rsrc(ws + OFF_AT, (unsigned)((size_t)D_ * L_ * 2));
    __builtin_amdgcn_raw_buffer_store_b128(o, ars, d * 4096 + tid * 16, 0, 16);
  }
}

DI float gelu_t(float u) {
  const float p = fmaf(u * u, 0.10294324f, 2.3022082f);
  const float e = __builtin_amdgcn_exp2f(u * p);
  return fmaf(-u, __builtin_amdgcn_rcpf(e + 1.f), u);
}
template <int OFF2>
DI bf16x8 tr_frag(const char* ad) {
  s16x4 lo = __builtin_amdgcn_ds_read_tr16_b64_v4i16((LAS s16x4*)(ad));
  s16x4 hi = __builtin_amdgcn_ds_read_tr16_b64_v4i16((LAS s16x4*)(ad + OFF2));
  return __builtin_shufflevector(lo, hi, 0, 1, 2, 3, 4, 5, 6, 7);
}

DI void gemm1_block(int bid, const u16* __restrict__ w1, const u16* __restrict__ b1, char* __restrict__ ws, char* lds,
                    ConvIn& cin, int cd0, const u16* __restrict__ dsk, const u16* __restrict__ w2) {
  constexpr int R = G1_R;
  constexpr int WD = 4;
  constexpr int NST = 16;
  const int xcd = bid & 7, loc = bid >> 3;
  const int ft = xcd * 9 + loc / 3, dg = loc % 3;
  const int tid = threadIdx.x, lane = tid & 63, gq = lane >> 4, q = (lane & 15) >> 2, p = lane & 3;
  const int wv = __builtin_amdgcn_readfirstlane(tid >> 6), dgp = wv & 3, kh = wv >> 2;
  const int dt0 = dg * 12 + dgp * 3;
  const int srow = tid >> 3, sch = tid & 7;
  const int ssw = ((srow >> 1) & 1) | (((srow >> 3) & 1) << 1);
  const int st_off = srow * 128 + ((sch ^ (ssw << 1)) * 16);
  const __amdgpu_buffer_rsrc_t wrs = mk_rsrc(w1 + ft * 64, 0x7fffffff);
  const int woff = (srow * F_ + sch * 8) * 2;
  constexpr int wst = 128 * F_ * 2, whalf = 64 * F_ * 2;
  const int fsw = (q >> 1) | ((gq & 1) << 1);
  const int rd_base = kh * 4096 + (8 * gq + q) * 128 + 8 * p;
  const char* frl = ws + OFF_HF + (size_t)dt0 * 64 * 1024 + kh * 1024 + lane * 16;
  const u32x2 ba = *(const u32x2*)(b1 + ft * 64 + 32 * kh + 4 * gq);
  const u32x2 bb = *(const u32x2*)(b1 + ft * 64 + 32 * kh + 16 + 4 * gq);
  f32x4 acc[4][3];
#pragma unroll
  for (int a = 0; a < 4; ++a)
#pragma unroll
    for (int b = 0; b < 3; ++b) acc[a][b] = f32x4{0, 0, 0, 0};
  u32x4 wr[WD][2];
  bf16x8 hb[R][3];
  bf16x8 wf[2][4];
  auto ldw = [&](int T, int h) -> u32x4 { return __builtin_amdgcn_raw_buffer_load_b128(wrs, woff + T * wst + h * whalf, 0, W1_AUX); };
  auto ldh = [&](int i, int b) -> bf16x8 { return *(const bf16x8*)(frl + (size_t)b * 64 * 1024 + (size_t)i * 2048); };
  auto rdw = [&](int i, int a) -> bf16x8 { return tr_frag<512>(lds + ((i >> 1) % 3) * 16384 + (i & 1) * 8192 + rd_base + ((a ^ fsw) * 32)); };
#pragma unroll
  for (int i = 0; i < WD; ++i) { wr[i][0] = ldw(i, 0); wr[i][1] = ldw(i, 1); }
#pragma unroll
  for (int j = 0; j < R - 1; ++j)
#pragma unroll
    for (int b = 0; b < 3; ++b) hb[j][b] = ldh(j, b);
#pragma unroll
  for (int i = 0; i < 2; ++i) {
    *(u32x4*)(lds + i * 16384 + st_off) = wr[i][0];
    *(u32x4*)(lds + i * 16384 + 8192 + st_off) = wr[i][1];
    wr[i][0] = ldw(i + WD, 0); wr[i][1] = ldw(i + WD, 1);
  }
  u32 pfh[2] = {0, 0};
  __syncthreads();
#pragma unroll
  for (int a = 0; a < 4; ++a) wf[0][a] = rdw(0, a);
#pragma unroll
  for (int i = 0; i < 32; ++i) {
    const int T = i >> 1, h = i & 1;
#pragma unroll
    for (int a = 0; a < 4; ++a) {
      SB();
#pragma unroll
      for (int b = 0; b < 3; ++b) acc[a][b] = mfma_bf(wf[i & 1][a], hb[i % R][b], acc[a][b]);
      if (i + 1 < 32) wf[(i + 1) & 1][a] = rdw(i + 1, a);
      if (a < 3) { if (i + R - 1 < 32) hb[(i + R - 1) % R][a] = ldh(i + R - 1, a); }
      if (a == 3) {
        if (T + 2 < NST) {
          *(u32x4*)(lds + ((T + 2) % 3) * 16384 + h * 8192 + st_off) = wr[(T + 2) % WD][h];
          if (T + 2 + WD < NST) wr[(T + 2) % WD][h] = ldw(T + 2 + WD, h);
        }
      }
    }
    if (i == CONV_LD_AT) { if (cd0 >= 0) conv_load(cin, cd0, dsk, ws); }
    SB();
    if (h == 1) __syncthreads();
  }
  asm volatile("" ::"v"(pfh[0]), "v"(pfh[1]));
  f32x4* red = (f32x4*)lds;
#pragma unroll
  for (int a2 = 0; a2 < 2; ++a2)
#pragma unroll
    for (int b = 0; b < 3; ++b) red[((wv * 2 + a2) * 3 + b) * 64 + lane] = kh ? acc[a2][b] : acc[2 + a2][b];
  __syncthreads();
  const float bia[4] = {bflo(ba[0]), bfhi(ba[0]), bflo(ba[1]), bfhi(ba[1])};
  const float bib[4] = {bflo(bb[0]), bfhi(bb[0]), bflo(bb[1]), bfhi(bb[1])};
  const __amdgpu_buffer_rsrc_t hrs = mk_rsrc(ws + OFF_HH, (unsigned)((size_t)36 * 144 * 1024));
#pragma unroll
  for (int b = 0; b < 3; ++b) {
    const f32x4 oa = red[(((wv ^ 4) * 2 + 0) * 3 + b) * 64 + lane], ob = red[(((wv ^ 4) * 2 + 1) * 3 + b) * 64 + lane];
    const f32x4 sa = (kh ? acc[2][b] : acc[0][b]) + oa, sb = (kh ? acc[3][b] : acc[1][b]) + ob;
    float ra[4], rb[4];
#pragma unroll
    for (int i = 0; i < 4; ++i) {
      ra[i] = gelu_t(rbf(rbf(sa[i]) + bia[i]));
      rb[i] = gelu_t(rbf(rbf(sb[i]) + bib[i]));
    }
    u32x4 o = {pk2(ra[0], ra[1]), pk2(ra[2], ra[3]), pk2(rb[0], rb[1]), pk2(rb[2], rb[3])};
    __builtin_amdgcn_raw_buffer_store_b128(o, hrs, (((dt0 + b) * 144 + ft * 2 + kh) * 64 + lane) * 16, 0, 16);
  }
}

DI void publish(gu32* ctr) {
  asm volatile("s_waitcnt vmcnt(0)" ::: "memory");
  __syncthreads();
  if (threadIdx.x < NREP) __hip_atomic_fetch_add(ctr + 64 * threadIdx.x, 1u, RLX_AGENT);
}
DI void wait_ge(gu32* word, unsigned want, gu32* tmo) {
  if (threadIdx.x == 0) {
    for (unsigned spins = 0;;) {
      if (__hip_atomic_load(word, RLX_AGENT) >= want) break;
      __builtin_amdgcn_s_sleep(1);
      if (++spins > (1u << 20)) { __hip_atomic_store(tmo, 1u, RLX_AGENT); break; }
      if ((spins & 1023u) == 0u && __hip_atomic_load(tmo, RLX_AGENT) != 0u) break;
    }
    __builtin_amdgcn_fence(__ATOMIC_ACQUIRE, "wavefront");
  }
  __syncthreads();
}

DI void gemm2_block(int st, int dgi, const u16* __restrict__ w2, const u16* __restrict__ b2, char* __restrict__ ws,
                    u16* __restrict__ out, char* lds) {
  constexpr int RW = 2, RH = G2_RH8, NS = 18, NDT = 5;
  gu32* ctl = (gu32*)(ws + OFF_CTL);
  const int tid = threadIdx.x, lane = tid & 63, gq = lane >> 4, q = (lane & 15) >> 2, p = lane & 3, c = lane & 15;
  const int wv = __builtin_amdgcn_readfirstlane(tid >> 6);
  const int d0 = dgi * 72;
  const int dtb = d0 >> 4;
  const bool odd = dgi & 1;
  constexpr int LD = G2_LD;
  char* my = lds + wv * 18432;
  const int srow = lane >> 3, sch = lane & 7;
  const __amdgpu_buffer_rsrc_t wrs = mk_rsrc(w2 + st * 64, 0x7fffffff);
  const int woff = ((wv * 32 + srow) * L_ + sch * 8) * 2;
  constexpr int wstep = 256 * L_ * 2, wrow8 = 8 * L_ * 2;
  const int st_off = srow * 128 + ((sch ^ (((srow >> 1) & 3) << 1)) * 16);
  const int fsw = (q >> 1) | ((gq & 1) << 1);
  const int rd_base = (4 * gq + q) * 128 + 8 * p;
  const __amdgpu_buffer_rsrc_t hrs = mk_rsrc(ws + OFF_HH, (unsigned)((size_t)36 * 144 * 1024));
  const int hoff = (dtb * 144 + wv) * 1024 + lane * 16;
  const int inv0 = (odd && c < 8) ? 0x40000000 : 0;
  const int inv4 = (!odd && c >= 8) ? 0x40000000 : 0;
  f32x4 acc[4][NDT];
#pragma unroll
  for (int a = 0; a < 4; ++a)
#pragma unroll
    for (int b = 0; b < NDT; ++b) acc[a][b] = f32x4{0, 0, 0, 0};
  u32x4 wr[RW][4];
  bf16x8 hf[RH][NDT];
  bf16x8 wf[2][4];
    auto ldw = [&](int i, int r) -> u32x4 { return __builtin_amdgcn_raw_buffer_load_b128(wrs, woff + i * wstep + r * wrow8, 0, W2_AUX); };
  auto ldh = [&](int i, int b) -> bf16x8 {
    const int o = hoff + b * 144 * 1024 + i * 8192 + (b == 0 ? inv0 : (b == 4 ? inv4 : 0));
    return __builtin_bit_cast(bf16x8, __builtin_amdgcn_raw_buffer_load_b128(hrs, o, 0, 16));
  };
  auto stw = [&](int i, int r, u32x4 v) { *(u32x4*)(my + (i % LD) * 4096 + r * 1024 + st_off) = v; };
  auto rdw = [&](int i, int a) -> bf16x8 { return tr_frag<2048>(my + (i % LD) * 4096 + rd_base + ((a ^ fsw) * 32)); };
#pragma unroll
  for (int i = 0; i < RW; ++i)
#pragma unroll
    for (int r = 0; r < 4; ++r) wr[i][r] = ldw(i, r);
  float* sg = (float*)(lds + 157824);
  if (tid < 64) sg[tid] = ((const float*)(ws + OFF_G))[st * 64 + tid];
  else if (tid < 128) sg[tid] = bf2f(b2[st * 64 + tid - 64]);
#pragma unroll
  for (int i = 0; i < LD - 1; ++i)
#pragma unroll
    for (int r = 0; r < 4; ++r) { stw(i, r, wr[i % RW][r]); wr[i % RW][r] = ldw(i + RW, r); }
  wait_ge(ctl + W_HFC + 64 * (blockIdx.x % NREP), 216u, ctl + W_TMO);
#pragma unroll
  for (int i = 0; i < RH - 1; ++i)
#pragma unroll
    for (int b = 0; b < NDT; ++b) hf[i][b] = ldh(i, b);
#pragma unroll
  for (int a = 0; a < 4; ++a) wf[0][a] = rdw(0, a);
  const __amdgpu_buffer_rsrc_t ars = mk_rsrc(ws + OFF_AT, (unsigned)((size_t)D_ * L_ * 2));
  unsigned acv = 0; bool a_ok = false;
  u32x4 av0 = {0, 0, 0, 0}, av1 = {0, 0, 0, 0};
#pragma unroll
  for (int i = 0; i < NS; ++i) {
#pragma unroll
    for (int a = 0; a < 4; ++a) {
      SB();
#pragma unroll
      for (int b = 0; b < NDT; ++b) acc[a][b] = mfma_bf(hf[i % RH][b], wf[i & 1][a], acc[a][b]);
      if (i + 1 < NS) wf[(i + 1) & 1][a] = rdw(i + 1, a);
      if (i + LD - 1 < NS) {
        stw(i + LD - 1, a, wr[(i + LD - 1) % RW][a]);
        if (i + LD - 1 + RW < NS) wr[(i + LD - 1) % RW][a] = ldw(i + LD - 1 + RW, a);
      }
      if (i + RH - 1 < NS) {
        hf[(i + RH - 1) % RH][a] = ldh(i + RH - 1, a);
        if (a == 3) hf[(i + RH - 1) % RH][4] = ldh(i + RH - 1, 4);
      }
    }
    SB();
    if (i == 13) acv = __hip_atomic_load(ctl + W_AC + 64 * (blockIdx.x % NREP), RLX_AGENT);
    if (i == 15) {
      a_ok = __builtin_amdgcn_readfirstlane(acv) >= 288u;
      __builtin_amdgcn_fence(__ATOMIC_ACQUIRE, "wavefront");
      if (a_ok) {
        av0 = __builtin_amdgcn_raw_buffer_load_b128(ars, (d0 + wv * 9 + (lane >> 3)) * 4096 + st * 128 + (lane & 7) * 16, 0, 16);
        if (lane < 8) av1 = __builtin_amdgcn_raw_buffer_load_b128(ars, (d0 + wv * 9 + 8) * 4096 + st * 128 + lane * 16, 0, 16);
      }
    }
  }
  char* as = lds + 147456;
  if (!a_ok) {
    for (unsigned spins = 0;;) {
      if (__builtin_amdgcn_readfirstlane(__hip_atomic_load(ctl + W_AC + 64 * (blockIdx.x % NREP), RLX_AGENT)) >= 288u) break;
      __builtin_amdgcn_s_sleep(POLL_SLEEP);
      if (++spins > (1u << 20)) { __hip_atomic_store(ctl + W_TMO, 1u, RLX_AGENT); break; }
      if ((spins & 1023u) == 0u && __builtin_amdgcn_readfirstlane(__hip_atomic_load(ctl + W_TMO, RLX_AGENT)) != 0u) break;
    }
    __builtin_amdgcn_fence(__ATOMIC_ACQUIRE, "wavefront");
    av0 = __builtin_amdgcn_raw_buffer_load_b128(ars, (d0 + wv * 9 + (lane >> 3)) * 4096 + st * 128 + (lane & 7) * 16, 0, 16);
    if (lane < 8) av1 = __builtin_amdgcn_raw_buffer_load_b128(ars, (d0 + wv * 9 + 8) * 4096 + st * 128 + lane * 16, 0, 16);
  }
  *(u32x4*)(as + (wv * 9 + (lane >> 3)) * 144 + (lane & 7) * 16) = av0;
  if (lane < 8) *(u32x4*)(as + (wv * 9 + 8) * 144 + lane * 16) = av1;
#pragma unroll
  for (int T = 0; T < 20; ++T) {
    const int own = T & 7;
    if (own != wv) {
      const int slot = T - ((T > wv) + (T > wv + 8) + (T > wv + 16));
      *(f32x4*)(my + slot * 1024 + lane * 16) = acc[T / 5][T % 5];
    }
  }
  LBAR();
#pragma unroll
  for (int T = 0; T < 20; ++T) {
    if ((T & 7) == wv) {
      f32x4 s = acc[T / 5][T % 5];
#pragma unroll
      for (int w = 0; w < 8; ++w) {
        if (w != wv) {
          const int slot = T - ((T > w) + (T > w + 8) + (T > w + 16));
          s += *(const f32x4*)(lds + w * 18432 + slot * 1024 + lane * 16);
        }
      }
      const int a = T / 5, b = T % 5;
      const int sl = 16 * a + c, sidx = st * 64 + sl;
      const int dl = 16 * (dtb + b) + 4 * gq - d0;
      if (dl >= 0 && dl < 72) {
        const float gg = sg[sl], og = rbf(1.f - gg), bb = sg[64 + sl];
        float o[4];
#pragma unroll
        for (int i = 0; i < 4; ++i) {
          const float av = bf2f(*(const u16*)(as + (dl + i) * 144 + sl * 2));
          const float bv = rbf(rbf(s[i]) + bb);
          o[i] = rbf(gg * av) + rbf(og * bv);
        }
        u32x2 ov = {pk2(o[0], o[1]), pk2(o[2], o[3])};
        *(u32x2*)(out + (size_t)sidx * D_ + d0 + dl) = ov;
      }
    }
  }
}

constexpr int K2_LDS = 2 * CONV_LDS;
__global__ __launch_bounds__(512) void k2_main(const u16* __restrict__ w1, const u16* __restrict__ b1, const u16* __restrict__ w2,
                                               const u16* __restrict__ b2, const u16* __restrict__ dsk, char* __restrict__ ws,
                                               u16* __restrict__ out) {
  __shared__ __attribute__((aligned(16))) char lds[158336];
  const int bid = blockIdx.x;
  gu32* ctl = (gu32*)(ws + OFF_CTL);
  if (bid < 216) {
    ConvIn cin;
    gemm1_block(bid, w1, b1, ws, lds, cin, bid < 168 ? 2 * (120 + bid) : -1, dsk, w2);
    publish(ctl + W_HFC);
    u32 pf[2] = {0, 0};
    {
      const __amdgpu_buffer_rsrc_t rs = mk_rsrc(w2, (unsigned)((size_t)L_ * F_ * 2));
      const int xg = bid & 7, j = bid >> 3;
#pragma unroll
      for (int i = 0; i < 2; ++i) {
        const int ln = (i * 512 + (int)threadIdx.x) * 27 + j;
        pf[i] = __builtin_amdgcn_raw_buffer_load_b32(rs, ln < 18432 ? ((ln >> 2) * L_ + xg * 256 + (ln & 3) * 64) * 2 : 0x7ffffff0, 0, PF_AUX);
      }
    }
    if (bid < 168) { conv_pair(cin, 2 * (120 + bid), ws, lds); asm volatile("" ::"v"(pf[0]), "v"(pf[1])); publish(ctl + W_AC); }
    else asm volatile("" ::"v"(pf[0]), "v"(pf[1]));
  } else {
    ConvIn ca, cb, cc;
    const int p0 = (bid - 216) * 3;
    conv_load(ca, 2 * p0, dsk, ws);
    conv_load(cb, 2 * (p0 + 1), dsk, ws);
    conv_load(cc, 2 * (p0 + 2), dsk, ws);
    asm volatile("" ::: "memory");
    SB();
    u32 pfa[8] = {0, 0, 0, 0, 0, 0, 0, 0};
    conv_pair(ca, 2 * p0, ws, lds); LBAR();
    conv_pair(cb, 2 * (p0 + 1), ws, lds); LBAR();
    conv_pair(cc, 2 * (p0 + 2), ws, lds);
#pragma unroll
    for (int i = 0; i < 8; ++i) asm volatile("" ::"v"(pfa[i]));
    asm volatile("s_waitcnt vmcnt(0)" ::: "memory");
    __syncthreads();
    if (threadIdx.x < NREP) __hip_atomic_fetch_add(ctl + W_AC + 64 * threadIdx.x, 3u, RLX_AGENT);
  }
  const int xcd = bid & 7, loc = bid >> 3;
  gemm2_block(xcd * 4 + (loc >> 3), loc & 7, w2, b2, ws, out, lds);
}

extern "C" void kernel_launch(void* const* d_in, const int* in_sizes, int n_in, void* d_out, int out_size, void* d_ws,
                              size_t ws_size, hipStream_t stream) {
  const u16* x = (const u16*)d_in[0];
  const u16* lar = (const u16*)d_in[1];
  const u16* aim = (const u16*)d_in[2];
  const u16* cr = (const u16*)d_in[3];
  const u16* ci = (const u16*)d_in[4];
  const u16* ldt = (const u16*)d_in[5];
  const u16* dsk = (const u16*)d_in[6];
  const u16* w1 = (const u16*)d_in[8];
  const u16* b1 = (const u16*)d_in[9];
  const u16* w2 = (const u16*)d_in[10];
  const u16* b2 = (const u16*)d_in[11];
  const u16* gw = (const u16*)d_in[12];
  char* ws = (char*)d_ws;
  k1_pre<<<256 + 576, 256, 0, stream>>>(x, gw, lar, aim, cr, ci, ldt, w1, w2, ws);
    k2_main<<<256, 512, 0, stream>>>(w1, b1, w2, b2, dsk, ws, (u16*)d_out);
}
```
